```python
import torch

def _softmax(x, axis=-1):
    return torch.softmax(x, dim=axis)
def _gelu(x):
    return torch.nn.functional.gelu(x, approximate="tanh")
def _silu(x):
    return torch.nn.functional.silu(x)
def _sigmoid(x):
    return torch.sigmoid(x)
def _layernorm(x, axis=-1, eps=1e-5):
    m = x.mean(dim=axis, keepdim=True)
    v = x.var(dim=axis, keepdim=True, unbiased=False)
    return (x - m) / torch.sqrt(v + eps)
def _scatter_add(tgt, idx, src, axis=0):
    return tgt.clone().index_add_(axis, idx, src)
def _segment_sum(src, seg, n):
    out = torch.zeros((n,) + src.shape[1:], dtype=src.dtype)
    return out.index_add_(0, seg, src)
def _pad(x, pw):
    flat = tuple(v for pair in reversed(pw) for v in pair)
    return torch.nn.functional.pad(x, flat)

kernel_name = 'parallel_sum__compressed_kv_cla__dense_activation_squared_relu__prompt'

def setup_inputs():

    _w = (lambda a: (lambda t: t.to(torch.bfloat16) if t.is_floating_point() else t)(torch.as_tensor(a)))
    def _A_setup():
        B, H, Sq, Sk, D, M = 1, 32, 2048, 2048, 128, 512
        Dc = 4 * D
        return {
            "x": _w((lambda *s: torch.randn(*s, dtype=torch.bfloat16) * 0.05)(B, Sq, M)),
            "c_kv": _w((lambda *s: torch.randn(*s, dtype=torch.bfloat16) * 1.6)(B, Sk, Dc)),
            "w_q": _w((lambda *s: torch.randn(*s, dtype=torch.bfloat16) * 1.6)(M, H, D) * (M ** -0.5)),
            "w_uk": _w((lambda *s: torch.randn(*s, dtype=torch.bfloat16) * 1.6)(Dc, H, D) * (Dc ** -0.5)),
            "w_uv": _w((lambda *s: torch.randn(*s, dtype=torch.bfloat16) * 1.6)(Dc, H, D) * (Dc ** -0.5)),
            "w_o": _w((lambda *s: torch.randn(*s, dtype=torch.bfloat16) * 1.6)(H, D, M) * ((H * D) ** -0.5)),
        }
    _A_in = _A_setup()

    def _B_setup():
        B, S, D, F = 1, 2048, 512, 1792
        return {
            "x": _w((lambda *s: torch.randn(*s, dtype=torch.bfloat16) * 0.05)(B, S, D)),
            "w1": _w((lambda *s: torch.randn(*s, dtype=torch.bfloat16) * 0.05)(D, F)),
            "b1": _w((lambda *s: torch.randn(*s, dtype=torch.bfloat16) * 0.4)(F)),
            "w2": _w((lambda *s: torch.randn(*s, dtype=torch.bfloat16) * 0.05)(F, D)),
            "b2": _w((lambda *s: torch.randn(*s, dtype=torch.bfloat16) * 1.6)(D)),
        }
    _B_in = _B_setup()

    return {
        "A_x": _A_in["x"],
        "A_c_kv": _A_in["c_kv"],
        "A_w_q": _A_in["w_q"],
        "A_w_uk": _A_in["w_uk"],
        "A_w_uv": _A_in["w_uv"],
        "A_w_o": _A_in["w_o"],
        "B_x": _B_in["x"],
        "B_w1": _B_in["w1"],
        "B_b1": _B_in["b1"],
        "B_w2": _B_in["w2"],
        "B_b2": _B_in["b2"],
    }

def reference(A_x, A_c_kv, A_w_q, A_w_uk, A_w_uv, A_w_o, B_x, B_w1, B_b1, B_w2, B_b2):
    def _A_ref(x, c_kv, w_q, w_uk, w_uv, w_o):
        B, Sq, M = x.shape[0], x.shape[1], x.shape[2]
        Sk = c_kv.shape[1]
        H, D = w_q.shape[1], w_q.shape[2]
        q = torch.einsum("bqm,mhd->bhqd", x, w_q)
        k = torch.einsum("bkc,chd->bhkd", c_kv, w_uk)
        v = torch.einsum("bkc,chd->bhkd", c_kv, w_uv)
        scores = torch.einsum("bhqd,bhkd->bhqk", q, k) * (D ** -0.5)
        off = Sk - Sq
        qi = torch.arange(Sq)[:, None]
        kj = torch.arange(Sk)[None, :]
        cmask = kj <= (qi + off)
        scores = torch.where(cmask[None, None], scores, -1e9)
        p = _softmax(scores, axis=-1)
        ctx = torch.einsum("bhqk,bhkd->bhqd", p, v)
        return torch.einsum("bhqd,hdm->bqm", ctx, w_o)

    def _B_ref(x, w1, b1, w2, b2):
        h = torch.einsum("bsd,df->bsf", x, w1) + b1
        zero = torch.zeros_like(h)
        r = torch.where(h > zero, h, zero)
        h = r * r
        out = torch.einsum("bsf,fd->bsd", h, w2) + b2
        return out

    x = A_x
    xn = _layernorm(x, axis=-1)
    return x + _A_ref(xn, A_c_kv, A_w_q, A_w_uk, A_w_uv, A_w_o) + _B_ref(xn, B_w1, B_b1, B_w2, B_b2)
```

```cpp
#include <hip/hip_runtime.h>
#include <hip/hip_bf16.h>
#include <stdint.h>
#include <type_traits>

typedef unsigned short u16;
typedef short bf16x8 __attribute__((ext_vector_type(8)));
typedef short s16x4 __attribute__((ext_vector_type(4)));
typedef short v4i16_t __attribute__((ext_vector_type(4)));
typedef float f32x16 __attribute__((ext_vector_type(16)));
typedef float f32x4 __attribute__((ext_vector_type(4)));
typedef unsigned u32x4 __attribute__((ext_vector_type(4)));
typedef unsigned u32x2 __attribute__((ext_vector_type(2)));
#define LAS __attribute__((address_space(3)))
#define DI __device__ __forceinline__
#define MFMA(a, b, c) __builtin_amdgcn_mfma_f32_32x32x16_bf16(a, b, c, 0, 0, 0)

constexpr int SEQ = 2048, DM = 512, NH = 32, HD = 128, FF = 1792;
constexpr int KST = 368;
constexpr float QSCALE = 0.08838834764831845f * 1.4426950408889634f;
constexpr float THR = 8.0f;

constexpr size_t WS_XN = 0;
constexpr size_t WS_K = 2u << 20;
constexpr size_t WS_V = WS_K + (16u << 20);
constexpr size_t WS_X = WS_V + (16u << 20);
constexpr size_t WS_P = WS_X + (size_t)64 * KST * 1024;
constexpr size_t WS_END = WS_P + ((size_t)32 << 20);

template <int I, int N, class F> DI void sfor(F&& f) {
    if constexpr (I < N) { f(std::integral_constant<int, I>{}); sfor<I + 1, N>(f); }
}
typedef float f32x2 __attribute__((ext_vector_type(2)));
typedef __bf16 bf16x2_t __attribute__((ext_vector_type(2)));
DI unsigned cvtpk(float lo, float hi) { f32x2 v = {lo, hi}; return __builtin_bit_cast(unsigned, __builtin_convertvector(v, bf16x2_t)); }
DI float bf_lo(unsigned u) { return __uint_as_float(u << 16); }
DI float bf_hi(unsigned u) { return __uint_as_float(u & 0xffff0000u); }
DI float rbf(float f) { return bf_lo(cvtpk(f, f)); }
DI void glds16(unsigned voff, const void* sbase, unsigned lds_dst) {
    unsigned sv;
    asm volatile("s_mov_b32 %0, m0\n\ts_mov_b32 m0, %3\n\ts_nop 0\n\tglobal_load_lds_dwordx4 %1, %2\n\ts_mov_b32 m0, %0"
                 : "=&s"(sv) : "v"(voff), "s"(sbase), "s"(lds_dst) : "memory");
}
template <int OFF> DI void gld16(bf16x8& dst, unsigned voff, const void* sbase) {
    asm volatile("global_load_dwordx4 %0, %1, %2 offset:%3" : "=v"(dst) : "v"(voff), "s"(sbase), "i"(OFF) : "memory");
}
template <int OFF> DI void gld8(u32x2& dst, unsigned voff, const void* sbase) {
    asm volatile("global_load_dwordx2 %0, %1, %2 offset:%3" : "=v"(dst) : "v"(voff), "s"(sbase), "i"(OFF) : "memory");
}
template <int N> DI void waitv() { asm volatile("s_waitcnt vmcnt(%0)" :: "i"(N) : "memory"); }
template <int N> DI void waitv4(bf16x8& a, bf16x8& b, bf16x8& c, bf16x8& d) {
    asm volatile("s_waitcnt vmcnt(%4)" : "+v"(a), "+v"(b), "+v"(c), "+v"(d) : "i"(N) : "memory");
}
DI void bar() { asm volatile("s_waitcnt lgkmcnt(0)\n\ts_barrier" ::: "memory"); }
DI s16x4 trrd(LAS const char* p) { return __builtin_bit_cast(s16x4, __builtin_amdgcn_ds_read_tr16_b64_v4i16((LAS v4i16_t*)p)); }
DI bf16x8 trfrag(LAS const char* p, int off_lo, int off_hi) {
    s16x4 a = trrd(p + off_lo), b = trrd(p + off_hi);
    return (bf16x8){a[0], a[1], a[2], a[3], b[0], b[1], b[2], b[3]};
}
DI bf16x8 ldsfrag(LAS const char* p) { return *(LAS const bf16x8*)p; }
template <class T> DI T* opaque(T* p) { asm volatile("" : "+v"(p)); return p; }
DI bf16x8 pack8(const f32x16& v, int b, float s) {
    u32x4 w = {cvtpk(v[b] * s, v[b + 1] * s), cvtpk(v[b + 2] * s, v[b + 3] * s), cvtpk(v[b + 4] * s, v[b + 5] * s), cvtpk(v[b + 6] * s, v[b + 7] * s)};
    return __builtin_bit_cast(bf16x8, w);
}
DI bf16x8 pack8n(const f32x16& v, int b) {
    u32x4 w = {cvtpk(v[b], v[b + 1]), cvtpk(v[b + 2], v[b + 3]), cvtpk(v[b + 4], v[b + 5]), cvtpk(v[b + 6], v[b + 7])};
    return __builtin_bit_cast(bf16x8, w);
}
DI float xhalf_max(float v) {
    auto rr = __builtin_amdgcn_permlane32_swap(__float_as_uint(v), __float_as_uint(v), false, false);
    return fmaxf(__uint_as_float(rr[0]), __uint_as_float(rr[1]));
}
DI float xhalf_sum(float v) {
    auto rr = __builtin_amdgcn_permlane32_swap(__float_as_uint(v), __float_as_uint(v), false, false);
    return __uint_as_float(rr[0]) + __uint_as_float(rr[1]);
}

__global__ __launch_bounds__(512, 2) void k_kv(const u16* __restrict__ x, const u16* __restrict__ ckv, const u16* __restrict__ wuk,
                                               const u16* __restrict__ wuv, u16* __restrict__ xn, u16* __restrict__ Kf, u16* __restrict__ Vf) {
    __shared__ __attribute__((aligned(1024))) char lds_[65536];
    const int tid = threadIdx.x, lane = tid & 63, r32 = lane & 31, hi = lane >> 5;
    const int w = __builtin_amdgcn_readfirstlane(tid >> 6);
    const int b = blockIdx.x, xcd = b & 7, idx = b >> 3, h = xcd * 4 + (idx & 3), j = idx >> 2;
    const unsigned lds0 = (unsigned)(uintptr_t)lds_;
    LAS const char* lds = (LAS const char*)lds_;
    bf16x8 xrow;
    gld16<0>(xrow, (unsigned)(lane * 16), (const char*)x + (size_t)(b * 8 + w) * 1024);
    const unsigned wvoff = (unsigned)((8 * w + ((lane >> 2) & 7)) * 8192 + (lane >> 5) * 64 + (lane & 3) * 16);
    const unsigned avoff = (unsigned)((32 * w + r32) * 1024 + hi * 16);
    const char* abase = (const char*)ckv + (size_t)j * 256 * 1024;
    const char* wkb = (const char*)wuk + h * 256;
    const char* wvb = (const char*)wuv + h * 256;
    const unsigned ldst = lds0 + w * 2048;
    bf16x8 a[32];
    auto issue = [&](auto ic) __attribute__((always_inline)) {
        constexpr int st = ic;
        const char* src = (st < 8 ? wkb : wvb) + (size_t)(st & 7) * 64 * 8192;
        glds16(wvoff, src, ldst + (st & 3) * 16384);
        glds16(wvoff, src + 128, ldst + (st & 3) * 16384 + 1024);
        if constexpr (st < 8) {
            gld16<(4 * st + 0) * 32>(a[4 * st + 0], avoff, abase);
            gld16<(4 * st + 1) * 32>(a[4 * st + 1], avoff, abase);
            gld16<(4 * st + 2) * 32>(a[4 * st + 2], avoff, abase);
            gld16<(4 * st + 3) * 32>(a[4 * st + 3], avoff, abase);
        }
    };
    sfor<0, 3>(issue);
    const int g16 = lane >> 4, q4 = (lane >> 2) & 3, p4 = lane & 3;
    LAS const char* rb = lds + hi * 2048 + q4 * 64 + (g16 & 1) * 32 + p4 * 8;
    f32x16 acc[4];
    const int rg = 8 * j + w;
    sfor<0, 16>([&](auto ic) __attribute__((always_inline)) {
        constexpr int st = ic;
        constexpr int cnt = st <= 5 ? 12 : st == 6 ? 8 : st == 7 ? 4 : st <= 10 ? 12 : st <= 13 ? 4 : st == 14 ? 2 : 0;
        if constexpr (st < 8) waitv4<cnt>(a[4 * st], a[4 * st + 1], a[4 * st + 2], a[4 * st + 3]); else waitv<cnt>();
        bar();
        if constexpr (st + 3 < 16) issue(std::integral_constant<int, st + 3>{});
        if constexpr ((st & 7) == 0) { acc[0] = f32x16{}; acc[1] = f32x16{}; acc[2] = f32x16{}; acc[3] = f32x16{}; }
#pragma unroll
        for (int u = 0; u < 4; ++u)
#pragma unroll
            for (int nt = 0; nt < 4; ++nt) {
                const int off = (st & 3) * 16384 + u * 4096 + nt * 512;
                bf16x8 wf = trfrag(rb, off, off + 256);
                if constexpr (st < 8) acc[nt] = MFMA(wf, a[4 * (st & 7) + u], acc[nt]);
                else acc[nt] = MFMA(a[4 * (st & 7) + u], wf, acc[nt]);
            }
        if constexpr (st == 7) {
            bf16x8* dst = (bf16x8*)(Kf + ((size_t)(h * 64 + rg) * 8) * 512) + lane;
#pragma unroll
            for (int nt = 0; nt < 4; ++nt) { dst[(2 * nt) * 64] = pack8n(acc[nt], 0); dst[(2 * nt + 1) * 64] = pack8n(acc[nt], 8); }
        }
        if constexpr (st == 15) {
            bf16x8* dst = (bf16x8*)(Vf + ((size_t)(h * 64 + rg) * 8) * 512) + lane;
#pragma unroll
            for (int nt = 0; nt < 4; ++nt) { dst[nt * 64] = pack8n(acc[nt], 0); dst[(4 + nt) * 64] = pack8n(acc[nt], 8); }
        }
    });
    {
        asm volatile("" : "+v"(xrow));
        u32x4 xu = __builtin_bit_cast(u32x4, xrow);
        float v[8];
#pragma unroll
        for (int e = 0; e < 4; ++e) { v[2 * e] = bf_lo(xu[e]); v[2 * e + 1] = bf_hi(xu[e]); }
        float s = 0.f;
#pragma unroll
        for (int e = 0; e < 8; ++e) s += v[e];
#pragma unroll
        for (int o = 32; o >= 1; o >>= 1) s += __shfl_xor(s, o);
        const float mean = s * (1.0f / DM);
        float q = 0.f;
#pragma unroll
        for (int e = 0; e < 8; ++e) { const float d = v[e] - mean; q += d * d; }
#pragma unroll
        for (int o = 32; o >= 1; o >>= 1) q += __shfl_xor(q, o);
        const float mb = rbf(mean), vb = rbf(q * (1.0f / DM));
        const float sd = rbf(__builtin_sqrtf(rbf(vb + 1e-5f)));
        u32x4 o;
#pragma unroll
        for (int e = 0; e < 4; ++e) o[e] = cvtpk(rbf(v[2 * e] - mb) / sd, rbf(v[2 * e + 1] - mb) / sd);
        *((u32x4*)(xn + (size_t)(b * 8 + w) * 512) + lane) = o;
    }
}

constexpr int A_K = 0, A_V = 65536, A_Q = 131072, A_LDS = 163840;

DI void attn_tile(LAS const char* kb, LAS const char* vb, const bf16x8 (&qf)[8], f32x16 (&o)[4], float& m, float& l, bool mask, int dq) {
    f32x16 p0 = f32x16{}, p1 = f32x16{};
#pragma unroll
    for (int ks = 0; ks < 8; ++ks) {
        bf16x8 k0 = ldsfrag(kb + ks * 1024), k1 = ldsfrag(kb + 8192 + ks * 1024);
        p0 = MFMA(k0, qf[ks], p0);
        p1 = MFMA(k1, qf[ks], p1);
    }
    if (mask) {
        const float NEG = -__builtin_inff();
#pragma unroll
        for (int r = 0; r < 16; ++r) {
            const int c = (r & 3) + 8 * (r >> 2);
            if (c > dq) p0[r] = NEG;
            if (c + 32 > dq) p1[r] = NEG;
        }
    }
    float pm = fmaxf(p0[0], p1[0]);
#pragma unroll
    for (int r = 1; r < 16; ++r) pm = fmaxf(fmaxf(pm, p0[r]), p1[r]);
    pm = xhalf_max(pm);
    if (__builtin_expect(!__all(pm - m <= THR), 0)) {
        const float mn = fmaxf(m, pm), al = __builtin_amdgcn_exp2f(m - mn);
        m = mn; l *= al;
#pragma unroll
        for (int d = 0; d < 4; ++d)
#pragma unroll
            for (int r = 0; r < 16; ++r) o[d][r] *= al;
    }
    float s = 0.f;
#pragma unroll
    for (int r = 0; r < 16; ++r) { p0[r] = __builtin_amdgcn_exp2f(p0[r] - m); s += p0[r]; }
#pragma unroll
    for (int r = 0; r < 16; ++r) { p1[r] = __builtin_amdgcn_exp2f(p1[r] - m); s += p1[r]; }
    l += s;
    bf16x8 pf[4] = {pack8n(p0, 0), pack8n(p0, 8), pack8n(p1, 0), pack8n(p1, 8)};
#pragma unroll
    for (int k2 = 0; k2 < 4; ++k2)
#pragma unroll
        for (int d = 0; d < 4; ++d) o[d] = MFMA(ldsfrag(vb + (k2 * 4 + d) * 1024), pf[k2], o[d]);
}

__global__ __launch_bounds__(512, 2) void k_attn(const u16* __restrict__ xn, const u16* __restrict__ wq, const u16* __restrict__ w1,
                                                 const u16* __restrict__ b1, const u16* __restrict__ Kf, const u16* __restrict__ Vf,
                                                 u16* __restrict__ Xf) {
    __shared__ __attribute__((aligned(1024))) char lds_[A_LDS];
    const int tid = threadIdx.x, lane = tid & 63, r32 = lane & 31, hi = lane >> 5;
    const int w = __builtin_amdgcn_readfirstlane(tid >> 6);
    const int b = blockIdx.x, xcd = b & 7, idx = b >> 3, h = xcd * 4 + (idx & 3), pi = idx >> 2;
    const int grp = w >> 2, wr = w & 3;
    const int qbig = 15 - pi, qsm = pi;
    const unsigned lds0 = (unsigned)(uintptr_t)lds_;
    LAS char* lds = (LAS char*)lds_;
    const int NT = 2 * (16 - pi), n1 = 2 * pi + 2;
    const int hcol = h < 28 ? h : h - 28;
    const int rg_own = (grp == 0 ? qbig : qsm) * 4 + wr;
    const unsigned wvoff = (unsigned)((8 * w + ((lane >> 2) & 7)) * 8192 + (lane >> 5) * 64 + (lane & 3) * 16);
    const unsigned hvoff = (unsigned)((16 * w + ((lane >> 2) & 7)) * (FF * 2) + (lane >> 5) * 64 + (lane & 3) * 16);
    const unsigned avoff = (unsigned)((rg_own * 32 + r32) * 1024 + hi * 16);
    const unsigned tvoff = (unsigned)(w * 2048 + lane * 16);
    const char* wqb = (const char*)wq + h * 256;
    const char* w1b = (const char*)w1 + hcol * 128;
    const char* kgb = (const char*)Kf + (size_t)h * 32 * 16384;
    const char* vgb = (const char*)Vf + (size_t)h * 32 * 16384;
    const unsigned ldst = lds0 + w * 2048;
    auto issue_tile = [&](int t) __attribute__((always_inline)) {
        const unsigned so = (unsigned)(t & 3) * 16384;
        glds16(tvoff, kgb + (size_t)t * 16384, ldst + A_K + so);
        glds16(tvoff, kgb + (size_t)t * 16384 + 1024, ldst + A_K + so + 1024);
        glds16(tvoff, vgb + (size_t)t * 16384, ldst + A_V + so);
        glds16(tvoff, vgb + (size_t)t * 16384 + 1024, ldst + A_V + so + 1024);
    };
    bf16x8 qf[8];
    {
        bf16x8 a[32];
        u32x2 bias[8];
        auto issue = [&](auto ic) __attribute__((always_inline)) {
            constexpr int st = ic;
            if constexpr (st < 8) {
                const char* src = wqb + (size_t)st * 64 * 8192;
                glds16(wvoff, src, ldst + A_V + (st & 3) * 16384);
                glds16(wvoff, src + 128, ldst + A_V + (st & 3) * 16384 + 1024);
                gld16<(4 * st + 0) * 32>(a[4 * st + 0], avoff, xn);
                gld16<(4 * st + 1) * 32>(a[4 * st + 1], avoff, xn);
                gld16<(4 * st + 2) * 32>(a[4 * st + 2], avoff, xn);
                gld16<(4 * st + 3) * 32>(a[4 * st + 3], avoff, xn);
            } else if constexpr (st < 12) {
                const char* src = w1b + (size_t)(st - 8) * 128 * (FF * 2);
                glds16(hvoff, src, ldst + A_V + (st & 3) * 16384);
                glds16(hvoff, src + 8 * (FF * 2), ldst + A_V + (st & 3) * 16384 + 1024);
                if constexpr (st == 11) {
                    const char* bsrc = (const char*)b1 + hcol * 128;
                    sfor<0, 8>([&](auto e) __attribute__((always_inline)) { gld8<16 * (int)e>(bias[(int)e], (unsigned)(hi * 8), bsrc); });
                }
            } else {
                issue_tile(st - 12);
            }
        };
        sfor<0, 3>(issue);
        const int g16 = lane >> 4, q4 = (lane >> 2) & 3, p4 = lane & 3;
        LAS const char* rbq = opaque(lds + A_V + hi * 2048 + q4 * 64 + (g16 & 1) * 32 + p4 * 8);
        LAS const char* rbh = opaque(lds + A_V + hi * 1024 + q4 * 64 + (g16 & 1) * 32 + p4 * 8);
        f32x16 acc[4];
        sfor<0, 12>([&](auto ic) __attribute__((always_inline)) {
            constexpr int st = ic;
            constexpr int cnt = st <= 5 ? 12 : st == 6 ? 8 : st <= 8 ? 4 : st == 9 ? 12 : st == 10 ? 14 : 8;
            if constexpr (st < 8) waitv4<cnt>(a[4 * st], a[4 * st + 1], a[4 * st + 2], a[4 * st + 3]); else waitv<cnt>();
            bar();
            issue(std::integral_constant<int, st + 3>{});
            if constexpr (st == 0) { acc[0] = f32x16{}; acc[1] = f32x16{}; acc[2] = f32x16{}; acc[3] = f32x16{}; }
            if constexpr (st == 8) { acc[0] = f32x16{}; acc[1] = f32x16{}; }
            if constexpr (st < 8) {
#pragma unroll
                for (int u = 0; u < 4; ++u)
#pragma unroll
                    for (int nt = 0; nt < 4; ++nt) {
                        const int off = (st & 3) * 16384 + u * 4096 + nt * 512;
                        acc[nt] = MFMA(trfrag(rbq, off, off + 256), a[4 * st + u], acc[nt]);
                    }
            } else {
#pragma unroll
                for (int u = 0; u < 8; ++u)
#pragma unroll
                    for (int nt = 0; nt < 2; ++nt) {
                        const int off = (st & 3) * 16384 + u * 2048 + nt * 512;
                        acc[nt] = MFMA(trfrag(rbh, off, off + 256), a[8 * (st - 8) + u], acc[nt]);
                    }
            }
            if constexpr (st == 7) {
#pragma unroll
                for (int nt = 0; nt < 4; ++nt) { qf[2 * nt] = pack8(acc[nt], 0, QSCALE); qf[2 * nt + 1] = pack8(acc[nt], 8, QSCALE); }
            }
        });
        asm volatile("" : "+v"(bias[0]), "+v"(bias[1]), "+v"(bias[2]), "+v"(bias[3]), "+v"(bias[4]), "+v"(bias[5]), "+v"(bias[6]), "+v"(bias[7]));
#pragma unroll
        for (int nt = 0; nt < 2; ++nt)
#pragma unroll
            for (int g = 0; g < 4; ++g) {
                const u32x2 bb = bias[nt * 4 + g];
                const float bv[4] = {bf_lo(bb[0]), bf_hi(bb[0]), bf_lo(bb[1]), bf_hi(bb[1])};
#pragma unroll
                for (int e = 0; e < 4; ++e) { const float t = fmaxf(acc[nt][4 * g + e] + bv[e], 0.f); acc[nt][4 * g + e] = t * t; }
            }
        if (h < 28) {
            bf16x8* dst = (bf16x8*)(Xf + ((size_t)rg_own * KST + 256 + 4 * hcol) * 512) + lane;
#pragma unroll
            for (int nt = 0; nt < 2; ++nt) { dst[(2 * nt) * 64] = pack8n(acc[nt], 0); dst[(2 * nt + 1) * 64] = pack8n(acc[nt], 8); }
        }
    }
    f32x16 o[4] = {f32x16{}, f32x16{}, f32x16{}, f32x16{}};
    float m = -1e30f, l = 0.f;
    LAS const char* kb0 = lds + A_K + lane * 16;
    LAS const char* vb0 = opaque(lds + A_V + lane * 16);
    LAS char* qx = opaque(lds + A_Q + wr * 8192 + lane * 16);
    if (grp == 0) {
#pragma unroll
        for (int ks = 0; ks < 8; ++ks) *(LAS bf16x8*)(qx + ks * 1024) = qf[ks];
    }
    const int rg_big = qbig * 4 + wr;
    int row0 = rg_own * 32;
    int nissued = 3;
    for (int t = 0; t < NT;) {
        const bool ph1 = t < n1;
        if (ph1) waitv<8>(); else waitv<0>();
        bar();
        for (; nissued < t + 4 && nissued < NT; ++nissued) issue_tile(nissued);
        if (t == n1 && grp == 1) {
            const float il = 1.0f / xhalf_sum(l);
            bf16x8* dst = (bf16x8*)(Xf + ((size_t)rg_own * KST + 8 * h) * 512) + lane;
#pragma unroll
            for (int d = 0; d < 4; ++d) { dst[(2 * d) * 64] = pack8(o[d], 0, il); dst[(2 * d + 1) * 64] = pack8(o[d], 8, il); o[d] = f32x16{}; }
            m = -1e30f; l = 0.f;
#pragma unroll
            for (int ks = 0; ks < 8; ++ks) qf[ks] = *(LAS const bf16x8*)(qx + ks * 1024);
            row0 = rg_big * 32;
        }
        const int tt = ph1 ? t : t + grp, so = (tt & 3) * 16384;
        if (tt * 64 <= row0 + 31) attn_tile(kb0 + so, vb0 + so, qf, o, m, l, tt * 64 + 63 > row0, row0 + r32 - 64 * tt - 4 * hi);
        t += ph1 ? 1 : 2;
    }
    bar();
    l = xhalf_sum(l);
    {
        LAS char* mine = lds + w * 8192;
        LAS char* oth = lds + (w ^ 4) * 8192;
        LAS float* ml = (LAS float*)(lds + 65536);
#pragma unroll
        for (int dd = 0; dd < 2; ++dd)
#pragma unroll
            for (int g = 0; g < 4; ++g) {
                const f32x16& s = grp == 0 ? o[2 + dd] : o[dd];
                *(LAS f32x4*)(mine + (dd * 4 + g) * 1024 + lane * 16) = (f32x4){s[4 * g], s[4 * g + 1], s[4 * g + 2], s[4 * g + 3]};
            }
        ml[w * 128 + lane] = m; ml[w * 128 + 64 + lane] = l;
        bar();
        const float m2 = ml[(w ^ 4) * 128 + lane], l2 = ml[(w ^ 4) * 128 + 64 + lane];
        const float mn = fmaxf(m, m2), f1 = __builtin_amdgcn_exp2f(m - mn), f2 = __builtin_amdgcn_exp2f(m2 - mn);
        const float il = 1.0f / (l * f1 + l2 * f2), g1 = f1 * il, g2 = f2 * il;
        bf16x8* dst = (bf16x8*)(Xf + ((size_t)rg_big * KST + 8 * h + 4 * grp) * 512) + lane;
#pragma unroll
        for (int dd = 0; dd < 2; ++dd) {
            f32x16 s = grp == 0 ? o[dd] : o[2 + dd];
#pragma unroll
            for (int g = 0; g < 4; ++g) {
                const f32x4 t = *(LAS const f32x4*)(oth + (dd * 4 + g) * 1024 + lane * 16);
#pragma unroll
                for (int e = 0; e < 4; ++e) s[4 * g + e] = s[4 * g + e] * g1 + t[e] * g2;
            }
            dst[(2 * dd) * 64] = pack8n(s, 0); dst[(2 * dd + 1) * 64] = pack8n(s, 8);
        }
    }
}

__global__ __launch_bounds__(512, 2) void k_out(const u16* __restrict__ Xf, const u16* __restrict__ wo, const u16* __restrict__ w2, float* __restrict__ P) {
    __shared__ __attribute__((aligned(1024))) char lds_[65536];
    const int tid = threadIdx.x, lane = tid & 63, hi = lane >> 5;
    const int w = __builtin_amdgcn_readfirstlane(tid >> 6);
    const int b = blockIdx.x, xs = b & 7, idx = b >> 3, rb = idx >> 2, cb = idx & 3;
    const unsigned lds0 = (unsigned)(uintptr_t)lds_;
    LAS const char* lds = (LAS const char*)lds_;
    const unsigned wvoff = (unsigned)((8 * w + ((lane >> 2) & 7)) * 1024 + (lane >> 5) * 64 + (lane & 3) * 16);
    const unsigned wvoff_h = (unsigned)((8 * (w & 3) + ((lane >> 2) & 7)) * 1024 + (lane >> 5) * 64 + (lane & 3) * 16);
    const int rg = rb * 8 + w;
    const unsigned avoff = (unsigned)(rg * (KST * 1024) + lane * 16);
    const char* xa = (const char*)Xf + (size_t)xs * 32 * 1024;
    const char* xh = (const char*)Xf + (size_t)(256 + 14 * xs) * 1024;
    const char* wob = (const char*)wo + (size_t)xs * 512 * 1024 + cb * 256;
    const char* w2b = (const char*)w2 + (size_t)xs * 224 * 1024 + cb * 256;
    const unsigned ldst = lds0 + w * 2048;
    bf16x8 a[3][4];
    auto issueW = [&](auto ic) __attribute__((always_inline)) {
        constexpr int st = ic;
        const char* src = st < 8 ? wob + (size_t)st * 65536 : w2b + (size_t)(st - 8) * 65536;
        const unsigned vo = st == 11 ? wvoff_h : wvoff;
        glds16(vo, src, ldst + (st & 3) * 16384);
        glds16(vo, src + 128, ldst + (st & 3) * 16384 + 1024);
    };
    auto issueA = [&](auto ic) __attribute__((always_inline)) {
        constexpr int st = ic;
        const char* src = st < 8 ? xa + st * 4096 : xh + (st - 8) * 4096;
        gld16<0>(a[st % 3][0], avoff, src);
        gld16<1024>(a[st % 3][1], avoff, src);
        if constexpr (st < 11) { gld16<2048>(a[st % 3][2], avoff, src); gld16<3072>(a[st % 3][3], avoff, src); }
    };
    issueW(std::integral_constant<int, 0>{}); issueA(std::integral_constant<int, 0>{});
    issueW(std::integral_constant<int, 1>{}); issueA(std::integral_constant<int, 1>{});
    issueW(std::integral_constant<int, 2>{});
    const int g16 = lane >> 4, q4 = (lane >> 2) & 3, p4 = lane & 3;
    LAS const char* rbp = lds + hi * 256 + q4 * 64 + 8 * (2 * (g16 & 1) + (p4 >> 1) + 4 * (p4 & 1));
    f32x16 acc[4] = {f32x16{}, f32x16{}, f32x16{}, f32x16{}};
    sfor<0, 12>([&](auto ic) __attribute__((always_inline)) {
        constexpr int st = ic;
        constexpr int cnt = st <= 9 ? 8 : st == 10 ? 4 : 0;
        waitv4<cnt>(a[st % 3][0], a[st % 3][1], a[st % 3][2], a[st % 3][3]);
        bar();
        if constexpr (st + 2 < 12) issueA(std::integral_constant<int, st + 2>{});
        if constexpr (st + 3 < 12) issueW(std::integral_constant<int, st + 3>{});
        constexpr int NU = st == 11 ? 2 : 4;
#pragma unroll
        for (int u = 0; u < NU; ++u)
#pragma unroll
            for (int nt = 0; nt < 4; ++nt) {
                const int off = (st & 3) * 16384 + u * 4096 + nt * 512;
                acc[nt] = MFMA(trfrag(rbp, off, off + 2048), a[st % 3][u], acc[nt]);
            }
    });
    f32x4* dst = (f32x4*)(P + (((size_t)xs * 64 + rg) * 16 + cb * 4) * 1024) + lane;
#pragma unroll
    for (int nt = 0; nt < 4; ++nt)
#pragma unroll
        for (int g = 0; g < 4; ++g) dst[(nt * 4 + g) * 64] = (f32x4){acc[nt][4 * g], acc[nt][4 * g + 1], acc[nt][4 * g + 2], acc[nt][4 * g + 3]};
}

__global__ __launch_bounds__(256) void k_red(const float* __restrict__ P, const u16* __restrict__ x, const u16* __restrict__ b2, u16* __restrict__ out) {
    const int gid = blockIdx.x * 256 + threadIdx.x, lane = gid & 63, tile = gid >> 6, rg = tile >> 4, ct = tile & 15;
    const int row = rg * 32 + (lane & 31), col = ct * 32 + (lane >> 5) * 16;
    const f32x4* src = (const f32x4*)(P + (size_t)tile * 1024) + lane;
    f32x4 s[4];
#pragma unroll
    for (int g = 0; g < 4; ++g) s[g] = src[g * 64];
#pragma unroll
    for (int xs = 1; xs < 8; ++xs)
#pragma unroll
        for (int g = 0; g < 4; ++g) s[g] += src[(size_t)xs * (64 * 16 * 256) + g * 64];
    const u32x4* xp = (const u32x4*)(x + (size_t)row * 512 + col);
    const u32x4* bp = (const u32x4*)(b2 + col);
    u32x4 o[2];
#pragma unroll
    for (int hh = 0; hh < 2; ++hh) {
        const u32x4 xv = xp[hh], bv = bp[hh];
#pragma unroll
        for (int e = 0; e < 4; ++e) {
            const f32x4 sv = s[2 * hh + (e >> 1)];
            const float lo = sv[2 * (e & 1)] + bf_lo(xv[e]) + bf_lo(bv[e]);
            const float hi2 = sv[2 * (e & 1) + 1] + bf_hi(xv[e]) + bf_hi(bv[e]);
            o[hh][e] = cvtpk(lo, hi2);
        }
    }
    u32x4* op = (u32x4*)(out + (size_t)row * 512 + col);
    op[0] = o[0]; op[1] = o[1];
}

extern "C" void kernel_launch(void* const* d_in, const int* in_sizes, int n_in, void* d_out, int out_size, void* d_ws, size_t ws_size,
                              hipStream_t stream) {
    (void)in_sizes; (void)n_in; (void)out_size; (void)ws_size;
    const u16* x = (const u16*)d_in[0];
    const u16* ckv = (const u16*)d_in[1];
    const u16* wq = (const u16*)d_in[2];
    const u16* wuk = (const u16*)d_in[3];
    const u16* wuv = (const u16*)d_in[4];
    const u16* wo = (const u16*)d_in[5];
    const u16* w1 = (const u16*)d_in[7];
    const u16* b1 = (const u16*)d_in[8];
    const u16* w2 = (const u16*)d_in[9];
    const u16* b2 = (const u16*)d_in[10];
    char* ws = (char*)d_ws;
    u16* xn = (u16*)(ws + WS_XN);
    u16* Kf = (u16*)(ws + WS_K);
    u16* Vf = (u16*)(ws + WS_V);
    u16* Xf = (u16*)(ws + WS_X);
    float* P = (float*)(ws + WS_P);
    k_kv<<<256, 512, 0, stream>>>(x, ckv, wuk, wuv, xn, Kf, Vf);
    k_attn<<<256, 512, 0, stream>>>(xn, wq, w1, b1, Kf, Vf, Xf);
    k_out<<<256, 512, 0, stream>>>(Xf, wo, w2, P);
    k_red<<<256, 256, 0, stream>>>(P, x, b2, (u16*)d_out);
}
```

```cpp
#include <hip/hip_runtime.h>
#include <hip/hip_bf16.h>
#include <stdint.h>
#include <type_traits>

typedef unsigned short u16;
typedef short bf16x8 __attribute__((ext_vector_type(8)));
typedef short s16x4 __attribute__((ext_vector_type(4)));
typedef short v4i16_t __attribute__((ext_vector_type(4)));
typedef float f32x16 __attribute__((ext_vector_type(16)));
typedef float f32x4 __attribute__((ext_vector_type(4)));
typedef unsigned u32x4 __attribute__((ext_vector_type(4)));
typedef unsigned u32x2 __attribute__((ext_vector_type(2)));
#define LAS __attribute__((address_space(3)))
#define DI __device__ __forceinline__
#define MFMA(a, b, c) __builtin_amdgcn_mfma_f32_32x32x16_bf16(a, b, c, 0, 0, 0)

constexpr int SEQ = 2048, DM = 512, NH = 32, HD = 128, FF = 1792;
constexpr int KST = 368;
constexpr float QSCALE = 0.08838834764831845f * 1.4426950408889634f;
#define THRV 24.0f
constexpr float THR = THRV;

constexpr size_t WS_XN = 0;
constexpr size_t WS_K = 2u << 20;
constexpr size_t WS_V = WS_K + (16u << 20);
constexpr size_t WS_X = WS_V + (16u << 20);
constexpr size_t WS_P = WS_X + (size_t)64 * KST * 1024;
constexpr size_t WS_C = WS_P + ((size_t)32 << 20);
constexpr size_t WS_END = WS_C + 48 * 256;

#define STAMP(k, i) do {} while (0)
template <int I, int N, class F> DI void sfor(F&& f) {
    if constexpr (I < N) { f(std::integral_constant<int, I>{}); sfor<I + 1, N>(f); }
}
typedef float f32x2 __attribute__((ext_vector_type(2)));
typedef __bf16 bf16x2_t __attribute__((ext_vector_type(2)));
DI unsigned cvtpk(float lo, float hi) { f32x2 v = {lo, hi}; return __builtin_bit_cast(unsigned, __builtin_convertvector(v, bf16x2_t)); }
DI float bf_lo(unsigned u) { return __uint_as_float(u << 16); }
DI float bf_hi(unsigned u) { return __uint_as_float(u & 0xffff0000u); }
DI float rbf(float f) { return bf_lo(cvtpk(f, f)); }
DI void glds16(unsigned voff, const void* sbase, unsigned lds_dst) {
    unsigned sv;
    asm volatile("s_mov_b32 %0, m0\n\ts_mov_b32 m0, %3\n\ts_nop 2\n\tglobal_load_lds_dwordx4 %1, %2\n\ts_mov_b32 m0, %0"
                 : "=&s"(sv) : "v"(voff), "s"(sbase), "s"(lds_dst) : "memory");
}
template <int OFF> DI void glds16x2(unsigned voff, const void* sbase, unsigned lds_dst) {
    unsigned sv;
    asm volatile("s_mov_b32 %0, m0\n\ts_mov_b32 m0, %3\n\ts_nop 2\n\tglobal_load_lds_dwordx4 %1, %2 offset:%4\n\tglobal_load_lds_dwordx4 %1, %2 offset:%5\n\ts_mov_b32 m0, %0"
                 : "=&s"(sv) : "v"(voff), "s"(sbase), "s"(lds_dst), "i"(OFF), "i"(OFF + 1024) : "memory");
}
template <int OFF> DI void glds16o(unsigned voff, const void* sbase, unsigned lds_dst) {
    unsigned sv;
    asm volatile("s_mov_b32 %0, m0\n\ts_mov_b32 m0, %3\n\ts_nop 2\n\tglobal_load_lds_dwordx4 %1, %2 offset:%4\n\ts_mov_b32 m0, %0"
                 : "=&s"(sv) : "v"(voff), "s"(sbase), "s"(lds_dst), "i"(OFF) : "memory");
}
template <int OFF> DI void glds16x4(unsigned voff, const void* sbase, unsigned lds_dst) {
    unsigned sv;
    asm volatile("s_mov_b32 %0, m0\n\ts_mov_b32 m0, %3\n\ts_nop 2\n\tglobal_load_lds_dwordx4 %1, %2 offset:%4\n\tglobal_load_lds_dwordx4 %1, %2 offset:%5\n\t"
                 "global_load_lds_dwordx4 %1, %2 offset:%6\n\tglobal_load_lds_dwordx4 %1, %2 offset:%7\n\ts_mov_b32 m0, %0"
                 : "=&s"(sv) : "v"(voff), "s"(sbase), "s"(lds_dst), "i"(OFF), "i"(OFF + 1024), "i"(OFF + 2048), "i"(OFF + 3072) : "memory");
}
DI void glds4(unsigned voff, const void* sbase, unsigned lds_dst) {
    unsigned sv;
    asm volatile("s_mov_b32 %0, m0\n\ts_mov_b32 m0, %3\n\ts_nop 2\n\tglobal_load_lds_dword %1, %2\n\ts_mov_b32 m0, %0"
                 : "=&s"(sv) : "v"(voff), "s"(sbase), "s"(lds_dst) : "memory");
}
template <int OFF> DI void gld16(bf16x8& dst, unsigned voff, const void* sbase) {
    asm volatile("s_nop 4\n\tglobal_load_dwordx4 %0, %1, %2 offset:%3" : "=v"(dst) : "v"(voff), "s"(sbase), "i"(OFF) : "memory");
}
template <int OFF, class T> DI void gld16s(T& dst, unsigned voff, const void* sbase) {
    asm volatile("s_nop 4\n\tglobal_load_dwordx4 %0, %1, %2 offset:%3 sc1" : "=v"(dst) : "v"(voff), "s"(sbase), "i"(OFF) : "memory");
}
template <int OFF> DI void gld8(u32x2& dst, unsigned voff, const void* sbase) {
    asm volatile("s_nop 4\n\tglobal_load_dwordx2 %0, %1, %2 offset:%3" : "=v"(dst) : "v"(voff), "s"(sbase), "i"(OFF) : "memory");
}
template <int OFF, bool SC1 = false, class T> DI void gst16(unsigned voff, T v, void* sbase) {
    if constexpr (SC1) asm volatile("s_nop 4\n\tglobal_store_dwordx4 %0, %1, %2 offset:%3 sc1\n\ts_nop 1" :: "v"(voff), "v"(v), "s"(sbase), "i"(OFF) : "memory");
    else asm volatile("s_nop 4\n\tglobal_store_dwordx4 %0, %1, %2 offset:%3\n\ts_nop 1" :: "v"(voff), "v"(v), "s"(sbase), "i"(OFF) : "memory");
}
template <bool SC1 = false, class T> DI void gst_frag4(unsigned voff, void* sbase, T f0, T f1, T f2, T f3) {
    gst16<0, SC1>(voff, f0, sbase); gst16<1024, SC1>(voff, f1, sbase); gst16<2048, SC1>(voff, f2, sbase); gst16<3072, SC1>(voff, f3, sbase);
}
DI void arrive(unsigned* cnt) { __hip_atomic_fetch_add(cnt, 1u, __ATOMIC_RELAXED, __HIP_MEMORY_SCOPE_AGENT); }
DI void wait_count(unsigned* cnt, unsigned target) {
    for (unsigned spins = 0; spins < (1u << 20); ++spins) {
        if ((unsigned)__builtin_amdgcn_readfirstlane(__hip_atomic_load(cnt, __ATOMIC_RELAXED, __HIP_MEMORY_SCOPE_AGENT)) >= target) break;
        __builtin_amdgcn_s_sleep(2);
    }
    asm volatile("" ::: "memory");
}
DI int lane_fresh() { int l; asm volatile("v_mbcnt_lo_u32_b32 %0, -1, 0\n\tv_mbcnt_hi_u32_b32 %0, -1, %0" : "=v"(l)); return l; }
template <int N> DI void waitv() { asm volatile("s_waitcnt vmcnt(%0)" :: "i"(N) : "memory"); }
template <int N> DI void waitv4(bf16x8& a, bf16x8& b, bf16x8& c, bf16x8& d) {
    asm volatile("s_waitcnt vmcnt(%4)" : "+v"(a), "+v"(b), "+v"(c), "+v"(d) : "i"(N) : "memory");
}
DI void bar() { asm volatile("s_waitcnt lgkmcnt(0)\n\ts_barrier" ::: "memory"); }
#define SB() __builtin_amdgcn_sched_barrier(0)
#define PIN(x) asm volatile("" : "+v"(x))
DI void rawbar() { SB(); asm volatile("s_barrier" ::: "memory"); SB(); }
#define PDAV 4
constexpr int PDA = PDAV;
constexpr int PD = 3;
DI s16x4 trrd(LAS const char* p) { return __builtin_bit_cast(s16x4, __builtin_amdgcn_ds_read_tr16_b64_v4i16((LAS v4i16_t*)p)); }
DI bf16x8 trfrag(LAS const char* p, int off_lo, int off_hi) {
    s16x4 a = trrd(p + off_lo), b = trrd(p + off_hi);
    return (bf16x8){a[0], a[1], a[2], a[3], b[0], b[1], b[2], b[3]};
}
DI bf16x8 ldsfrag(LAS const char* p) { return *(LAS const bf16x8*)p; }
template <class T> DI T* opaque(T* p) { asm volatile("" : "+v"(p)); return p; }
DI bf16x8 pack8(const f32x16& v, int b, float s) {
    u32x4 w = {cvtpk(v[b] * s, v[b + 1] * s), cvtpk(v[b + 2] * s, v[b + 3] * s), cvtpk(v[b + 4] * s, v[b + 5] * s), cvtpk(v[b + 6] * s, v[b + 7] * s)};
    return __builtin_bit_cast(bf16x8, w);
}
DI bf16x8 pack8n(const f32x16& v, int b) {
    u32x4 w = {cvtpk(v[b], v[b + 1]), cvtpk(v[b + 2], v[b + 3]), cvtpk(v[b + 4], v[b + 5]), cvtpk(v[b + 6], v[b + 7])};
    return __builtin_bit_cast(bf16x8, w);
}
DI float xhalf_max(float v) {
    auto rr = __builtin_amdgcn_permlane32_swap(__float_as_uint(v), __float_as_uint(v), false, false);
    return fmaxf(__uint_as_float(rr[0]), __uint_as_float(rr[1]));
}
DI float xhalf_sum(float v) {
    auto rr = __builtin_amdgcn_permlane32_swap(__float_as_uint(v), __float_as_uint(v), false, false);
    return __uint_as_float(rr[0]) + __uint_as_float(rr[1]);
}

__global__ __launch_bounds__(512, 2) void k_kv(const u16* __restrict__ x, const u16* __restrict__ ckv, const u16* __restrict__ wuk,
                                               const u16* __restrict__ wuv, u16* __restrict__ xn, u16* __restrict__ Kf, u16* __restrict__ Vf,
                                               unsigned* __restrict__ cnt) {
    __shared__ __attribute__((aligned(1024))) char lds_[163840];
    const int tid = threadIdx.x, lane = tid & 63, r32 = lane & 31, hi = lane >> 5;
    const int w = __builtin_amdgcn_readfirstlane(tid >> 6);
    const int b = blockIdx.x, xcd = b & 7, idx = b >> 3, h = xcd * 4 + (idx & 3), j = idx >> 2;
    const unsigned lds0 = (unsigned)(uintptr_t)lds_;
    LAS const char* lds = (LAS const char*)lds_;
    STAMP(0, 0);
    bf16x8 xrow;
    gld16<0>(xrow, (unsigned)(lane * 16), (const char*)x + (size_t)(b * 8 + w) * 1024);
    const unsigned wvoff = (unsigned)((8 * w + ((lane >> 2) & 7)) * 8192 + (lane >> 5) * 64 + (lane & 3) * 16);
    const unsigned avoff0 = (unsigned)((lane >> 3) * 1024 + (((lane & 7) ^ (lane >> 4)) * 16));
    const unsigned avoff1 = (unsigned)((lane >> 3) * 1024 + (((lane & 7) ^ (4 + (lane >> 4))) * 16));
    const char* abase = (const char*)ckv + (size_t)(j * 256 + w * 32) * 1024;
    const char* wkb = (const char*)wuk + h * 256;
    const char* wvb = (const char*)wuv + h * 256;
    const unsigned ldst = lds0 + w * 2048, adst = lds0 + 65536 + w * 12288;
    bf16x8 a[32];
    auto issue_p = [&](auto ic, auto pc) __attribute__((always_inline)) {
        constexpr int st = ic, p = pc;
        if constexpr (st < 16) {
            if constexpr (p < 2) glds16(wvoff, (st < 8 ? wkb : wvb) + (size_t)(st & 7) * 64 * 8192 + p * 128, ldst + (st & 3) * 16384 + p * 1024);
            else if constexpr (st < 8) glds16((p & 1) ? avoff1 : avoff0, abase + st * 128 + (p - 2) * 8192, adst + (st % 3) * 4096 + (p - 2) * 1024);
        }
    };
    sfor<0, 3>([&](auto ic) __attribute__((always_inline)) { sfor<0, 6>([&](auto pc) __attribute__((always_inline)) { issue_p(ic, pc); }); });
    const int g16 = lane >> 4, q4 = (lane >> 2) & 3, p4 = lane & 3;
    LAS const char* rb = lds + hi * 2048 + q4 * 64 + (g16 & 1) * 32 + p4 * 8;
    LAS const char* ra[4];
#pragma unroll
    for (int u = 0; u < 4; ++u) ra[u] = opaque(lds + 65536 + w * 12288 + r32 * 128 + (((2 * u + hi) ^ ((r32 >> 1) & 7)) * 16));
    f32x16 acc[4];
    const int rg = 8 * j + w;
    bf16x8 wc[PD];
    auto rdf = [&](int st, int i) __attribute__((always_inline)) {
        const int off = (st & 3) * 16384 + (i >> 2) * 4096 + (i & 3) * 512;
        return trfrag(rb, off, off + 256);
    };
    waitv<12>(); rawbar();
    STAMP(0, 1);
#pragma unroll
    for (int u = 0; u < 4; ++u) a[u] = ldsfrag(ra[u]);
#pragma unroll
    for (int i = 0; i < PD; ++i) wc[i] = rdf(0, i);
    sfor<0, 16>([&](auto ic) __attribute__((always_inline)) {
        constexpr int st = ic;
        constexpr int cnt = st <= 5 ? 6 : st <= 7 ? 2 : st <= 9 ? 10 : st <= 13 ? 2 : 0;
        waitv<cnt>();
        rawbar();
        bf16x8 wf[16 + PD];
#pragma unroll
        for (int i = 0; i < PD; ++i) wf[i] = wc[i];
        sfor<0, 16>([&](auto iic) __attribute__((always_inline)) {
            constexpr int i = iic;
            if constexpr (i + PD < 16) wf[i + PD] = rdf(st, i + PD);
            else if constexpr (st < 15) wf[i + PD] = rdf(st + 1, i + PD - 16);
            if constexpr (st < 7 && i >= 8 && i < 12) a[4 * (st + 1) + i - 8] = ldsfrag(ra[i - 8] + ((st + 1) % 3) * 4096);
            const f32x16 cin = ((st & 7) == 0 && i < 4) ? f32x16{} : acc[i & 3];
            if constexpr (st < 8) acc[i & 3] = MFMA(wf[i], a[4 * (st & 7) + (i >> 2)], cin);
            else acc[i & 3] = MFMA(a[4 * (st & 7) + (i >> 2)], wf[i], cin);
            if constexpr (i >= 1 && i < 7) issue_p(std::integral_constant<int, st + 3>{}, std::integral_constant<int, i - 1>{});
            SB();
        });
#pragma unroll
        for (int i = 0; i < PD; ++i) wc[i] = wf[16 + i];
        if constexpr (st == 7) STAMP(0, 2);
        if constexpr (st == 15) STAMP(0, 3);
        if constexpr (st == 7) {
            char* dst = (char*)Kf + (size_t)(h * 64 + rg) * 8192;
            gst_frag4((unsigned)(lane * 16), dst, pack8n(acc[0], 0), pack8n(acc[0], 8), pack8n(acc[1], 0), pack8n(acc[1], 8));
            gst_frag4((unsigned)(lane * 16), dst + 4096, pack8n(acc[2], 0), pack8n(acc[2], 8), pack8n(acc[3], 0), pack8n(acc[3], 8));
        }
        if constexpr (st == 15) {
            char* dst = (char*)Vf + (size_t)(h * 64 + rg) * 8192;
            gst_frag4((unsigned)(lane * 16), dst, pack8n(acc[0], 0), pack8n(acc[1], 0), pack8n(acc[2], 0), pack8n(acc[3], 0));
            gst_frag4((unsigned)(lane * 16), dst + 4096, pack8n(acc[0], 8), pack8n(acc[1], 8), pack8n(acc[2], 8), pack8n(acc[3], 8));
        }
    });
    {
        asm volatile("" : "+v"(xrow));
        u32x4 xu = __builtin_bit_cast(u32x4, xrow);
        float v[8];
#pragma unroll
        for (int e = 0; e < 4; ++e) { v[2 * e] = bf_lo(xu[e]); v[2 * e + 1] = bf_hi(xu[e]); }
        float s = 0.f;
#pragma unroll
        for (int e = 0; e < 8; ++e) s += v[e];
#pragma unroll
        for (int o = 32; o >= 1; o >>= 1) s += __shfl_xor(s, o);
        const float mean = s * (1.0f / DM);
        float q = 0.f;
#pragma unroll
        for (int e = 0; e < 8; ++e) { const float d = v[e] - mean; q += d * d; }
#pragma unroll
        for (int o = 32; o >= 1; o >>= 1) q += __shfl_xor(q, o);
        const float mb = rbf(mean), vb = rbf(q * (1.0f / DM));
        const float sd = rbf(__builtin_sqrtf(rbf(vb + 1e-5f)));
        u32x4 o;
#pragma unroll
        for (int e = 0; e < 4; ++e) o[e] = cvtpk(rbf(v[2 * e] - mb) / sd, rbf(v[2 * e + 1] - mb) / sd);
        const int row = b * 8 + w;
        *(u32x4*)((char*)xn + (size_t)(row >> 5) * 32768 + lane * 512 + (row & 31) * 16) = o;
    }
    if (b == 0 && tid < 48) cnt[tid * 64] = 0u;
    STAMP(0, 4);
}

constexpr int A_K = 0, A_V = 65536, A_Q = 131072, A_LDS = 163840;

DI float max3f(float a, float b, float c) { return __builtin_fmaxf(__builtin_fmaxf(a, b), c); }
DI void cmask(f32x16& p0, f32x16& p1, int dq) {
    const float NEG = -__builtin_inff();
#pragma unroll
    for (int r = 0; r < 16; ++r) {
        const int c = (r & 3) + 8 * (r >> 2);
        if (c > dq) p0[r] = NEG;
        if (c + 32 > dq) p1[r] = NEG;
    }
}

__global__ __launch_bounds__(512, 2) void k_main(const u16* __restrict__ xn, const u16* __restrict__ wq, const u16* __restrict__ w1,
                                                 const u16* __restrict__ b1, const u16* __restrict__ Kf, const u16* __restrict__ Vf,
                                                 u16* Xf, const u16* __restrict__ wo, const u16* __restrict__ w2, float* P,
                                                 unsigned* cnt, const u16* __restrict__ x, const u16* __restrict__ b2, u16* __restrict__ out) {
    __shared__ __attribute__((aligned(1024))) char lds_[A_LDS];
    const int tid = threadIdx.x, lane = tid & 63, r32 = lane & 31, hi = lane >> 5;
    const int w = __builtin_amdgcn_readfirstlane(tid >> 6);
    const int b = blockIdx.x, xcd = b & 7, idx = b >> 3, h = xcd * 4 + (idx & 3), pi = idx >> 2;
    const int grp = w >> 2, wr = w & 3;
    const int qbig = 15 - pi, qsm = pi;
    const unsigned lds0 = (unsigned)(uintptr_t)lds_;
    LAS char* lds = (LAS char*)lds_;
    const int n1 = 2 * pi + 2;
    const int hl = idx & 3, hc0 = 224 * xcd + (hl < 3 ? 64 * hl : 160);
    STAMP(1, 0);
    const int rg_own = (grp == 0 ? qbig : qsm) * 4 + wr;
    const unsigned wvoff = (unsigned)((8 * w + ((lane >> 2) & 7)) * 8192 + (lane >> 5) * 64 + (lane & 3) * 16);
    const unsigned hvoff = (unsigned)((16 * w + ((lane >> 2) & 7)) * (FF * 2) + (lane >> 5) * 64 + (lane & 3) * 16);
    const unsigned tvoff = (unsigned)(lane * 16);
    const char* abase = (const char*)xn + (size_t)rg_own * 32768;
    const char* wqb = (const char*)wq + h * 256;
    const char* w1b = (const char*)w1 + hc0 * 2;
    const char* kgb = (const char*)Kf + (size_t)h * 32 * 16384;
    const char* vgb = (const char*)Vf + (size_t)h * 32 * 16384;
    const unsigned ldst = lds0 + w * 2048;
    auto issue_step = [&](const char* gb, unsigned region, int s, auto pc) __attribute__((always_inline)) {
        constexpr int p = pc;
        if (s <= 16 && (p == 0 || s >= n1)) {
            const int off = s < n1 ? s * 16384 + w * 2048 : (2 * s - n1) * 16384 + w * 4096;
            glds16x2<2048 * p>(tvoff, gb + off, lds0 + region + (unsigned)(off & 65535));
        }
    };
    constexpr std::integral_constant<int, 0> P0{}; constexpr std::integral_constant<int, 1> P1{};
    bf16x8 qf[8];
    {
        bf16x8 a[32];
        auto issue_p = [&](auto ic, auto pc) __attribute__((always_inline)) {
            constexpr int st = ic, p = pc;
            if constexpr (st < 8) {
                if constexpr (p < 2) glds16(wvoff, wqb + (size_t)st * 64 * 8192 + p * 128, ldst + A_V + (st & 3) * 16384 + p * 1024);
                else gld16<(p - 2) * 1024>(a[4 * st + p - 2], tvoff, abase + st * 4096);
            } else if constexpr (st < 12) {
                if constexpr (p < 2) glds16(hvoff, w1b + (size_t)((st - 8) * 128 + 8 * p) * (FF * 2), ldst + A_V + (st & 3) * 16384 + p * 1024);
                if constexpr (st == 11 && p == 2) glds4((unsigned)(r32 * 4), (const char*)b1 + hc0 * 2, lds0 + A_K + 3 * 16384 + w * 256);
            } else if constexpr (st < 14) {
                if constexpr (p == 0) issue_step(kgb, A_K, st - 12, P0);
            }
        };
        sfor<0, 3>([&](auto ic) __attribute__((always_inline)) { sfor<0, 6>([&](auto pc) __attribute__((always_inline)) { issue_p(ic, pc); }); });
        const int g16 = lane >> 4, q4 = (lane >> 2) & 3, p4 = lane & 3;
        LAS const char* rbq = opaque(lds + A_V + hi * 2048 + q4 * 64 + (g16 & 1) * 32 + p4 * 8);
        LAS const char* rbh = opaque(lds + A_V + hi * 1024 + q4 * 64 + (g16 & 1) * 32 + p4 * 8);
        auto rdf = [&](int st, int i) __attribute__((always_inline)) {
            if (st < 8) { const int off = (st & 3) * 16384 + (i >> 2) * 4096 + (i & 3) * 512; return trfrag(rbq, off, off + 256); }
            else { const int off = (st & 3) * 16384 + (i >> 1) * 2048 + (i & 1) * 512; return trfrag(rbh, off, off + 256); }
        };
        f32x16 acc[4];
        bf16x8 wc[PD];
        waitv<12>(); rawbar();
        STAMP(1, 1);
#pragma unroll
        for (int i = 0; i < PD; ++i) wc[i] = rdf(0, i);
        sfor<0, 12>([&](auto ic) __attribute__((always_inline)) {
            constexpr int st = ic;
            constexpr int cnt = st <= 5 ? 6 : st <= 8 ? 2 : st == 9 ? 3 : st == 10 ? 2 : 4;
            if constexpr (st < 8) waitv4<cnt>(a[4 * st], a[4 * st + 1], a[4 * st + 2], a[4 * st + 3]); else waitv<cnt>();
            rawbar();
            bf16x8 wf[16 + PD];
#pragma unroll
            for (int i = 0; i < PD; ++i) wf[i] = wc[i];
            sfor<0, 16>([&](auto iic) __attribute__((always_inline)) {
                constexpr int i = iic;
                if constexpr (i + PD < 16) wf[i + PD] = rdf(st, i + PD);
                else if constexpr (st < 11) wf[i + PD] = rdf(st + 1, i + PD - 16);
                if constexpr (st < 8) {
                    const f32x16 cin = (st == 0 && i < 4) ? f32x16{} : acc[i & 3];
                    acc[i & 3] = MFMA(wf[i], a[4 * st + (i >> 2)], cin);
                } else {
                    const f32x16 cin = (st == 8 && i < 2) ? f32x16{} : acc[i & 1];
                    acc[i & 1] = MFMA(wf[i], a[8 * (st - 8) + (i >> 1)], cin);
                }
                if constexpr (i >= 1 && i < 7) issue_p(std::integral_constant<int, st + 3>{}, std::integral_constant<int, i - 1>{});
                SB();
            });
#pragma unroll
            for (int i = 0; i < PD; ++i) wc[i] = wf[16 + i];
            if constexpr (st == 7) {
#pragma unroll
                for (int nt = 0; nt < 4; ++nt) { qf[2 * nt] = pack8(acc[nt], 0, QSCALE); qf[2 * nt + 1] = pack8(acc[nt], 8, QSCALE); }
            }
        });
#pragma unroll
        for (int nt = 0; nt < 2; ++nt)
#pragma unroll
            for (int g = 0; g < 4; ++g) {
                const u32x2 bb = *(LAS const u32x2*)(lds + A_K + 3 * 16384 + w * 256 + hi * 8 + (nt * 4 + g) * 16);
                const float bv[4] = {bf_lo(bb[0]), bf_hi(bb[0]), bf_lo(bb[1]), bf_hi(bb[1])};
#pragma unroll
                for (int e = 0; e < 4; ++e) { const float t = fmaxf(acc[nt][4 * g + e] + bv[e], 0.f); acc[nt][4 * g + e] = t * t; }
            }
        char* hdst = (char*)Xf + ((size_t)rg_own * KST + 256 + (hc0 >> 4)) * 1024;
        if (hl < 3) { gst16<0, true>(tvoff, pack8n(acc[0], 0), hdst); gst16<1024, true>(tvoff, pack8n(acc[0], 8), hdst); }
        gst16<2048, true>(tvoff, pack8n(acc[1], 0), hdst); gst16<3072, true>(tvoff, pack8n(acc[1], 8), hdst);
    }
    LAS const char* kb0 = lds + A_K + lane * 16;
    LAS const char* vb0 = opaque(lds + A_V + lane * 16);
    LAS char* qx = opaque(lds + A_Q + wr * 8192 + lane * 16);
    if (grp == 0) {
#pragma unroll
        for (int ks = 0; ks < 8; ++ks) *(LAS bf16x8*)(qx + ks * 1024) = qf[ks];
    }
    const int rg_big = qbig * 4 + wr;
    f32x16 o[4] = {f32x16{}, f32x16{}, f32x16{}, f32x16{}};
    f32x16 negm = f32x16{};
    float mh = 0.f, l = 0.f, il_sm = 0.f;
    int row0 = rg_own * 32;
    f32x16 sA0, sA1, sB0, sB1;
    u32x4 pw[4];
    bf16x8 kc[PDA], vc[PDA];
    bool pend = false; float fpend = 1.f;
    auto tile_of = [&](int s) __attribute__((always_inline)) { return s < n1 ? s : 2 * s - n1 + grp; };
    auto decide = [&](f32x16& c0, f32x16& c1, float rm, bool first) __attribute__((always_inline)) {
        rm = xhalf_max(rm);
        if (__builtin_expect(__any(rm > THR) || first, 0)) {
            const float dl = first ? __builtin_fmaxf(rm, -1e30f) : __builtin_fmaxf(rm, 0.f);
            mh += dl;
#pragma unroll
            for (int r = 0; r < 16; ++r) { c0[r] -= dl; c1[r] -= dl; negm[r] = -mh; }
            fpend = first ? 0.f : __builtin_amdgcn_exp2f(-dl);
            l *= fpend; pend = true;
        }
    };
    STAMP(1, 2);
    waitv<2>(); bar();
    issue_step(vgb, A_V, 0, P0);
    {
#pragma unroll
        for (int ks = 0; ks < 8; ++ks) {
            const bf16x8 k0 = ldsfrag(kb0 + ks * 1024), k1 = ldsfrag(kb0 + 8192 + ks * 1024);
            sA0 = MFMA(k0, qf[ks], ks == 0 ? f32x16{} : sA0);
            sA1 = MFMA(k1, qf[ks], ks == 0 ? f32x16{} : sA1);
        }
        waitv<2>(); bar();
        issue_step(kgb, A_K, 2, P0); issue_step(kgb, A_K, 2, P1);
        if (63 > row0) cmask(sA0, sA1, row0 + r32 - 4 * hi);
        float rm = max3f(sA0[0], sA0[1], sA1[0]);
        rm = __builtin_fmaxf(rm, sA1[1]);
#pragma unroll
        for (int r = 2; r < 16; ++r) rm = max3f(rm, sA0[r], sA1[r]);
        decide(sA0, sA1, rm, true);
        pend = false;
#pragma unroll
        for (int r = 0; r < 16; ++r) { sA0[r] = __builtin_amdgcn_exp2f(sA0[r]); sA1[r] = __builtin_amdgcn_exp2f(sA1[r]); }
#pragma unroll
        for (int i = 0; i < PDA; ++i) kc[i] = ldsfrag(kb0 + 16384 + (i & 1) * 8192 + (i >> 1) * 1024);
        waitv<0>();
        bar();
        if (tid == 0) arrive(cnt + xcd * 64);
    }
#define PH(k) do {} while (0)
    auto step_off = [&](int s) __attribute__((always_inline)) {
        const int sc = s < 16 ? s : 16;
        return sc < n1 ? sc * 16384 + wr * 4096 : (2 * sc - n1) * 16384 + wr * 8192;
    };
    LAS const char* kb = kb0 + 16384;
    LAS const char* vb = vb0;
    LAS const char* kbn = kb0 + (tile_of(2) & 3) * 16384;
    auto step = [&](auto evn, f32x16& c0, f32x16& c1, f32x16& p0, f32x16& p1, const int s) __attribute__((always_inline)) {
        const bool sw = (bool)evn && (s == n1) && (grp == 1);
        const bool swn = !(bool)evn && (s + 1 == n1) && (grp == 1);
        const int voff_g = step_off(s), koff_g = step_off(s + 2);
        const unsigned vdst = lds0 + A_V + (unsigned)(voff_g & 65535);
        const unsigned kdst = lds0 + A_K + (unsigned)((s + 2 <= 16 ? koff_g : step_off(s)) & 65535);
        const bool vtwo = s >= n1, ktwo = s + 2 >= n1;
        const int tt = tile_of(s);
        PH(3);
        rawbar();
        PH(0);
        {
            bf16x8 kf[16];
#pragma unroll
            for (int i = 0; i < PDA; ++i) kf[i] = kc[i];
            float sa = 0.f, sb = 0.f;
            sfor<0, 16>([&](auto iic) __attribute__((always_inline)) {
                constexpr int i = iic;
                if constexpr (i + PDA < 16) kf[i + PDA] = ldsfrag(kb + ((i + PDA) & 1) * 8192 + ((i + PDA) >> 1) * 1024);
                else vc[i + PDA - 16] = ldsfrag(vb + (i + PDA - 16) * 1024);
                SB();
                if constexpr (i & 1) c1 = MFMA(kf[i], qf[i >> 1], i < 2 ? negm : c1);
                else c0 = MFMA(kf[i], qf[i >> 1], i < 2 ? negm : c0);
                const float e0 = i < 8 ? p0[2 * i] : p1[2 * i - 16], e1 = i < 8 ? p0[2 * i + 1] : p1[2 * i - 15];
                sa += e0; sb += e1;
                pw[i >> 2][i & 3] = cvtpk(e0, e1);
                PIN(sa); PIN(sb); PIN(pw[i >> 2]);
                SB();
            });
            l += sa + sb;
        }
        if (sw) { il_sm = 1.0f / xhalf_sum(l); l = 0.f; }
        PH(1);
        if (grp == 0) {
            waitv<0>();
            glds16x4<0>(tvoff, vgb + voff_g, vdst);
            if (vtwo) glds16x4<0>(tvoff, vgb + voff_g + 4096, vdst + 4096);
        }
        rawbar();
        PH(2);
        {
            bf16x8 vf[16];
#pragma unroll
            for (int i = 0; i < PDA; ++i) vf[i] = vc[i];
            float ra = 0.f, rb2 = 0.f;
            sfor<0, 16>([&](auto iic) __attribute__((always_inline)) {
                constexpr int i = iic;
                if constexpr (i + PDA < 16) vf[i + PDA] = ldsfrag(vb + (i + PDA) * 1024);
                else kc[i + PDA - 16] = ldsfrag(kbn + ((i + PDA - 16) & 1) * 8192 + ((i + PDA - 16) >> 1) * 1024);
                SB();
                o[i & 3] = MFMA(vf[i], __builtin_bit_cast(bf16x8, pw[i >> 2]), o[i & 3]);
                if constexpr (i == 0) {
                    if (tt * 64 + 63 > row0) { const int lf = lane_fresh(); cmask(c0, c1, row0 + (lf & 31) - 64 * tt - 4 * (lf >> 5)); }
                } else if constexpr (i < 5) {
                    constexpr int q = i - 1;
                    if constexpr (q == 0) { ra = max3f(c0[0], c0[1], c1[0]); rb2 = max3f(c0[2], c0[3], c1[1]); ra = max3f(ra, c1[2], c1[3]); }
                    else { ra = max3f(ra, c0[4 * q], c0[4 * q + 1]); rb2 = max3f(rb2, c0[4 * q + 2], c0[4 * q + 3]);
                           ra = max3f(ra, c1[4 * q], c1[4 * q + 1]); rb2 = max3f(rb2, c1[4 * q + 2], c1[4 * q + 3]); }
                    PIN(ra); PIN(rb2);
                } else if constexpr (i == 5) {
                    decide(c0, c1, __builtin_fmaxf(ra, rb2), sw);
                } else {
                    constexpr int e_lo = (32 * (i - 6)) / 10, e_hi = (32 * (i - 5)) / 10;
#pragma unroll
                    for (int e = e_lo; e < e_hi; ++e) {
                        if (e < 16) c0[e] = __builtin_amdgcn_exp2f(c0[e]); else c1[e - 16] = __builtin_amdgcn_exp2f(c1[e - 16]);
                    }
                    PIN(c0); PIN(c1);
                }
                if constexpr (i == 15 - PDA) {
                    vb = vb0 + (tt & 3) * 16384; PIN(vb);
                }
                if constexpr (i == 15) {
                    kb = kbn; kbn = kb0 + (tile_of(s + 2) & 3) * 16384; PIN(kb); PIN(kbn);
                }
                SB();
            });
        }
        if (sw) {
            char* dst = (char*)Xf + ((size_t)rg_own * KST + 8 * h) * 1024;
            gst_frag4<true>(tvoff, dst, pack8(o[0], 0, il_sm), pack8(o[0], 8, il_sm), pack8(o[1], 0, il_sm), pack8(o[1], 8, il_sm));
            gst_frag4<true>(tvoff, dst + 4096, pack8(o[2], 0, il_sm), pack8(o[2], 8, il_sm), pack8(o[3], 0, il_sm), pack8(o[3], 8, il_sm));
        }
        if (__builtin_expect(pend, 0)) {
#pragma unroll
            for (int d = 0; d < 4; ++d)
#pragma unroll
                for (int r = 0; r < 16; ++r) o[d][r] *= fpend;
            pend = false;
        }
        if (swn) {
#pragma unroll
            for (int ks = 0; ks < 8; ++ks) qf[ks] = *(LAS const bf16x8*)(qx + ks * 1024);
            negm = f32x16{}; mh = 0.f; row0 = rg_big * 32;
        }
        if (grp == 0) {
            waitv<0>();
            glds16x4<0>(tvoff, kgb + koff_g, kdst);
            if (ktwo) glds16x4<0>(tvoff, kgb + koff_g + 4096, kdst + 4096);
        }
    };
    STAMP(1, 3);
    for (int s = 1; s < 17; s += 2) {
        step(std::false_type{}, sB0, sB1, sA0, sA1, s);
        step(std::true_type{}, sA0, sA1, sB0, sB1, s + 1);
    }
    STAMP(1, 4);
    {
        const int lane = lane_fresh(), hi = lane >> 5;
        const int xs = xcd, rb = idx >> 2, cb = idx & 3;
        const unsigned wvo = (unsigned)((8 * w + ((lane >> 2) & 7)) * 1024 + (lane >> 5) * 64 + (lane & 3) * 16);
        const unsigned wvo_h = (unsigned)((8 * (w & 3) + ((lane >> 2) & 7)) * 1024 + (lane >> 5) * 64 + (lane & 3) * 16);
        const int rg = rb * 8 + w;
        const unsigned avo = (unsigned)(lane * 16);
        const char* xa = (const char*)Xf + (size_t)rg * (KST * 1024) + (size_t)xs * 32 * 1024;
        const char* xh = (const char*)Xf + (size_t)rg * (KST * 1024) + (size_t)(256 + 14 * xs) * 1024;
        const char* wob = (const char*)wo + (size_t)xs * 512 * 1024 + cb * 256;
        const char* w2b = (const char*)w2 + (size_t)xs * 224 * 1024 + cb * 256;
        bf16x8 a[3][4];
        auto issueW = [&](auto ic) __attribute__((always_inline)) {
            constexpr int st = ic;
            const char* src = st < 4 ? w2b + (size_t)st * 65536 : wob + (size_t)(st - 4) * 65536;
            const unsigned vo = st == 3 ? wvo_h : wvo;
            glds16(vo, src, ldst + A_K + (st & 3) * 16384);
            glds16(vo, src + 128, ldst + A_K + (st & 3) * 16384 + 1024);
        };
        auto issueA = [&](auto ic) __attribute__((always_inline)) {
            constexpr int st = ic;
            const char* src = st < 4 ? xh + st * 4096 : xa + (st - 4) * 4096;
            gld16s<0>(a[st % 3][0], avo, src);
            gld16s<1024>(a[st % 3][1], avo, src);
            if constexpr (st != 3) { gld16s<2048>(a[st % 3][2], avo, src); gld16s<3072>(a[st % 3][3], avo, src); }
        };
        if (w == 0) wait_count(cnt + xcd * 64, 32);
        waitv<0>(); bar();
        issueW(std::integral_constant<int, 0>{}); issueA(std::integral_constant<int, 0>{});
        issueW(std::integral_constant<int, 1>{}); issueA(std::integral_constant<int, 1>{});
        issueW(std::integral_constant<int, 2>{});
        {
            float sa = 0.f;
#pragma unroll
            for (int r = 0; r < 16; ++r) sa += sA0[r] + sA1[r];
            l += sa;
            const bf16x8 pf[4] = {pack8n(sA0, 0), pack8n(sA0, 8), pack8n(sA1, 0), pack8n(sA1, 8)};
#pragma unroll
            for (int i = 0; i < 16; ++i) o[i & 3] = MFMA(ldsfrag(vb + i * 1024), pf[i >> 2], o[i & 3]);
        }
        bar();
        l = xhalf_sum(l);
        {
            LAS char* mine = opaque(lds + A_V + w * 8192 + lane * 16);
            LAS char* oth = opaque(lds + A_V + (w ^ 4) * 8192 + lane * 16);
            LAS float* ml = opaque((LAS float*)(lds + A_Q) + lane);
#pragma unroll
            for (int dd = 0; dd < 2; ++dd)
#pragma unroll
                for (int g = 0; g < 4; ++g) {
                    const f32x16& sv = grp == 0 ? o[2 + dd] : o[dd];
                    *(LAS f32x4*)(mine + (dd * 4 + g) * 1024) = (f32x4){sv[4 * g], sv[4 * g + 1], sv[4 * g + 2], sv[4 * g + 3]};
                }
            ml[w * 128] = mh; ml[w * 128 + 64] = l;
            bar();
            const float m2 = ml[(w ^ 4) * 128], l2 = ml[(w ^ 4) * 128 + 64];
            const float mn = fmaxf(mh, m2), f1 = __builtin_amdgcn_exp2f(mh - mn), f2 = __builtin_amdgcn_exp2f(m2 - mn);
            const float il = 1.0f / (l * f1 + l2 * f2), g1 = f1 * il, g2 = f2 * il;
            f32x16 sv[2];
#pragma unroll
            for (int dd = 0; dd < 2; ++dd) {
                sv[dd] = grp == 0 ? o[dd] : o[2 + dd];
#pragma unroll
                for (int g = 0; g < 4; ++g) {
                    const f32x4 t = *(LAS const f32x4*)(oth + (dd * 4 + g) * 1024);
#pragma unroll
                    for (int e = 0; e < 4; ++e) sv[dd][4 * g + e] = sv[dd][4 * g + e] * g1 + t[e] * g2;
                }
            }
            gst_frag4<true>(avo, (char*)Xf + ((size_t)rg_big * KST + 8 * h + 4 * grp) * 1024,
                            pack8n(sv[0], 0), pack8n(sv[0], 8), pack8n(sv[1], 0), pack8n(sv[1], 8));
        }
        STAMP(1, 5);
        waitv<0>(); bar();
        if (tid == 0) arrive(cnt + (8 + xcd) * 64);
        const int g16 = lane >> 4, q4 = (lane >> 2) & 3, p4 = lane & 3;
        LAS const char* rbp = opaque(lds + A_K + hi * 256 + q4 * 64 + 8 * (2 * (g16 & 1) + (p4 >> 1) + 4 * (p4 & 1)));
        f32x16 acc[4];
        bf16x8 wc[PD];
        auto rdf = [&](int st, int i) __attribute__((always_inline)) {
            const int off = (st & 3) * 16384 + (i >> 2) * 4096 + (i & 3) * 512;
            return trfrag(rbp, off, off + 2048);
        };
#pragma unroll
        for (int i = 0; i < PD; ++i) wc[i] = rdf(0, i);
        sfor<0, 12>([&](auto ic) __attribute__((always_inline)) {
            constexpr int st = ic;
            constexpr int cnt_ = st == 2 ? 4 : st <= 9 ? 6 : st == 10 ? 4 : 0;
            if constexpr (st == 2) { if (w == 0) wait_count(cnt + (8 + xcd) * 64, 32); }
            waitv4<cnt_>(a[st % 3][0], a[st % 3][1], a[st % 3][2], a[st % 3][3]);
            rawbar();
            if constexpr (st + 2 < 12) issueA(std::integral_constant<int, st + 2>{});
            if constexpr (st + 3 < 12) issueW(std::integral_constant<int, st + 3>{});
            SB();
            constexpr int NF = st == 3 ? 8 : 16;
            bf16x8 wf[16 + PD];
#pragma unroll
            for (int i = 0; i < PD; ++i) wf[i] = wc[i];
#pragma unroll
            for (int i = 0; i < NF; ++i) {
                if (i + PD < NF) wf[i + PD] = rdf(st, i + PD);
                else if (st < 11) wf[i + PD] = rdf(st + 1, i + PD - NF);
                const f32x16 cin = (st == 0 && i < 4) ? f32x16{} : acc[i & 3];
                acc[i & 3] = MFMA(wf[i], a[st % 3][i >> 2], cin);
                SB();
            }
#pragma unroll
            for (int i = 0; i < PD; ++i) wc[i] = wf[NF + i];
        });
        STAMP(2, 0);
        char* pdst = (char*)P + (((size_t)xs * 64 + rg) * 16 + cb * 4) * 2048;
        gst_frag4<true>(avo, pdst, pack8n(acc[0], 0), pack8n(acc[0], 8), pack8n(acc[1], 0), pack8n(acc[1], 8));
        gst_frag4<true>(avo, pdst + 4096, pack8n(acc[2], 0), pack8n(acc[2], 8), pack8n(acc[3], 0), pack8n(acc[3], 8));
        waitv<0>(); bar();
        if (tid == 0) arrive(cnt + (16 + idx) * 64);
        const int row = (rb * 8 + xs) * 32 + (lane & 31), col = cb * 128 + (w >> 1) * 32 + hi * 16 + 8 * (w & 1);
        u32x4 xv = *(const u32x4*)(x + (size_t)row * 512 + col), bv = *(const u32x4*)(b2 + col);
        PIN(xv); PIN(bv);
        if (w == 0) wait_count(cnt + (16 + idx) * 64, 8);
        bar();
        STAMP(2, 1);
        {
            const int rgr = rb * 8 + xs;
            const char* src = (const char*)P + ((size_t)rgr * 16 + cb * 4) * 2048 + w * 1024;
            u32x4 pv[8];
#pragma unroll
            for (int k = 0; k < 8; ++k) gld16s<0>(pv[k], avo, src + (size_t)k * (64 * 16 * 2048));
            asm volatile("s_waitcnt vmcnt(0)" : "+v"(pv[0]), "+v"(pv[1]), "+v"(pv[2]), "+v"(pv[3]), "+v"(pv[4]), "+v"(pv[5]), "+v"(pv[6]), "+v"(pv[7]) :: "memory");
            u32x4 o4;
#pragma unroll
            for (int e = 0; e < 4; ++e) {
                float lo = bf_lo(xv[e]) + bf_lo(bv[e]), hh = bf_hi(xv[e]) + bf_hi(bv[e]);
#pragma unroll
                for (int k = 0; k < 8; ++k) { lo += bf_lo(pv[k][e]); hh += bf_hi(pv[k][e]); }
                o4[e] = cvtpk(lo, hh);
            }
            *(u32x4*)(out + (size_t)row * 512 + col) = o4;
        }
        STAMP(2, 2); STAMP(2, 3);
    }
}

extern "C" void kernel_launch(void* const* d_in, const int* in_sizes, int n_in, void* d_out, int out_size, void* d_ws, size_t ws_size,
                              hipStream_t stream) {
    (void)in_sizes; (void)n_in; (void)out_size; (void)ws_size;
    const u16* x = (const u16*)d_in[0];
    const u16* ckv = (const u16*)d_in[1];
    const u16* wq = (const u16*)d_in[2];
    const u16* wuk = (const u16*)d_in[3];
    const u16* wuv = (const u16*)d_in[4];
    const u16* wo = (const u16*)d_in[5];
    const u16* w1 = (const u16*)d_in[7];
    const u16* b1 = (const u16*)d_in[8];
    const u16* w2 = (const u16*)d_in[9];
    const u16* b2 = (const u16*)d_in[10];
    char* ws = (char*)d_ws;
    u16* xn = (u16*)(ws + WS_XN);
    u16* Kf = (u16*)(ws + WS_K);
    u16* Vf = (u16*)(ws + WS_V);
    u16* Xf = (u16*)(ws + WS_X);
    float* P = (float*)(ws + WS_P);
    unsigned* cnt = (unsigned*)(ws + WS_C);
    k_kv<<<256, 512, 0, stream>>>(x, ckv, wuk, wuv, xn, Kf, Vf, cnt);
    k_main<<<256, 512, 0, stream>>>(xn, wq, w1, b1, Kf, Vf, Xf, wo, w2, P, cnt, x, b2, (u16*)d_out);
}
```

```cpp
#include <hip/hip_runtime.h>
#include <hip/hip_bf16.h>
#include <stdint.h>
#include <type_traits>

typedef unsigned short u16;
typedef short bf16x8 __attribute__((ext_vector_type(8)));
typedef short s16x4 __attribute__((ext_vector_type(4)));
typedef short v4i16_t __attribute__((ext_vector_type(4)));
typedef float f32x16 __attribute__((ext_vector_type(16)));
typedef float f32x4 __attribute__((ext_vector_type(4)));
typedef unsigned u32x4 __attribute__((ext_vector_type(4)));
typedef unsigned u32x2 __attribute__((ext_vector_type(2)));
#define LAS __attribute__((address_space(3)))
#define DI __device__ __forceinline__
#define MFMA(a, b, c) __builtin_amdgcn_mfma_f32_32x32x16_bf16(a, b, c, 0, 0, 0)

constexpr int SEQ = 2048, DM = 512, NH = 32, HD = 128, FF = 1792;
constexpr int KST = 368;
constexpr float QSCALE = 0.08838834764831845f * 1.4426950408889634f;
#define THRV 24.0f
constexpr float THR = THRV;

constexpr size_t WS_XN = 0;
constexpr size_t WS_K = 2u << 20;
constexpr size_t WS_V = WS_K + (16u << 20);
constexpr size_t WS_X = WS_V + (16u << 20);
constexpr size_t WS_P = WS_X + (size_t)64 * KST * 1024;
constexpr size_t WS_C = WS_P + ((size_t)32 << 20);
constexpr size_t WS_END = WS_C + 48 * 256;

#define STAMP(k, i) do {} while (0)
template <int I, int N, class F> DI void sfor(F&& f) {
    if constexpr (I < N) { f(std::integral_constant<int, I>{}); sfor<I + 1, N>(f); }
}
typedef float f32x2 __attribute__((ext_vector_type(2)));
typedef __bf16 bf16x2_t __attribute__((ext_vector_type(2)));
DI unsigned cvtpk(float lo, float hi) { f32x2 v = {lo, hi}; return __builtin_bit_cast(unsigned, __builtin_convertvector(v, bf16x2_t)); }
DI float bf_lo(unsigned u) { return __uint_as_float(u << 16); }
DI float bf_hi(unsigned u) { return __uint_as_float(u & 0xffff0000u); }
DI float rbf(float f) { return bf_lo(cvtpk(f, f)); }
DI void glds16(unsigned voff, const void* sbase, unsigned lds_dst) {
    unsigned sv;
    asm volatile("s_mov_b32 %0, m0\n\ts_mov_b32 m0, %3\n\ts_nop 2\n\tglobal_load_lds_dwordx4 %1, %2\n\ts_mov_b32 m0, %0"
                 : "=&s"(sv) : "v"(voff), "s"(sbase), "s"(lds_dst) : "memory");
}
template <int OFF> DI void glds16x2(unsigned voff, const void* sbase, unsigned lds_dst) {
    unsigned sv;
    asm volatile("s_mov_b32 %0, m0\n\ts_mov_b32 m0, %3\n\ts_nop 2\n\tglobal_load_lds_dwordx4 %1, %2 offset:%4\n\tglobal_load_lds_dwordx4 %1, %2 offset:%5\n\ts_mov_b32 m0, %0"
                 : "=&s"(sv) : "v"(voff), "s"(sbase), "s"(lds_dst), "i"(OFF), "i"(OFF + 1024) : "memory");
}
template <int OFF> DI void glds16o(unsigned voff, const void* sbase, unsigned lds_dst) {
    unsigned sv;
    asm volatile("s_mov_b32 %0, m0\n\ts_mov_b32 m0, %3\n\ts_nop 2\n\tglobal_load_lds_dwordx4 %1, %2 offset:%4\n\ts_mov_b32 m0, %0"
                 : "=&s"(sv) : "v"(voff), "s"(sbase), "s"(lds_dst), "i"(OFF) : "memory");
}
template <int OFF> DI void glds16x4(unsigned voff, const void* sbase, unsigned lds_dst) {
    unsigned sv;
    asm volatile("s_mov_b32 %0, m0\n\ts_mov_b32 m0, %3\n\ts_nop 2\n\tglobal_load_lds_dwordx4 %1, %2 offset:%4\n\tglobal_load_lds_dwordx4 %1, %2 offset:%5\n\t"
                 "global_load_lds_dwordx4 %1, %2 offset:%6\n\tglobal_load_lds_dwordx4 %1, %2 offset:%7\n\ts_mov_b32 m0, %0"
                 : "=&s"(sv) : "v"(voff), "s"(sbase), "s"(lds_dst), "i"(OFF), "i"(OFF + 1024), "i"(OFF + 2048), "i"(OFF + 3072) : "memory");
}
template <int OFF> DI void gldsl(unsigned voff, const void* sbase, unsigned lds_dst) {
    asm volatile("s_mov_b32 m0, %2\n\ts_nop 0\n\tglobal_load_lds_dwordx4 %0, %1 offset:%3" :: "v"(voff), "s"(sbase), "s"(lds_dst), "i"(OFF) : "memory");
}
DI void glds4(unsigned voff, const void* sbase, unsigned lds_dst) {
    unsigned sv;
    asm volatile("s_mov_b32 %0, m0\n\ts_mov_b32 m0, %3\n\ts_nop 2\n\tglobal_load_lds_dword %1, %2\n\ts_mov_b32 m0, %0"
                 : "=&s"(sv) : "v"(voff), "s"(sbase), "s"(lds_dst) : "memory");
}
template <int OFF> DI void gld16(bf16x8& dst, unsigned voff, const void* sbase) {
    asm volatile("s_nop 4\n\tglobal_load_dwordx4 %0, %1, %2 offset:%3" : "=v"(dst) : "v"(voff), "s"(sbase), "i"(OFF) : "memory");
}
template <int OFF, class T> DI void gld16s(T& dst, unsigned voff, const void* sbase) {
    asm volatile("s_nop 4\n\tglobal_load_dwordx4 %0, %1, %2 offset:%3 sc1" : "=v"(dst) : "v"(voff), "s"(sbase), "i"(OFF) : "memory");
}
template <int OFF> DI void gld8(u32x2& dst, unsigned voff, const void* sbase) {
    asm volatile("s_nop 4\n\tglobal_load_dwordx2 %0, %1, %2 offset:%3" : "=v"(dst) : "v"(voff), "s"(sbase), "i"(OFF) : "memory");
}
template <int OFF, bool SC1 = false, class T> DI void gst16(unsigned voff, T v, void* sbase) {
    if constexpr (SC1) asm volatile("s_nop 4\n\tglobal_store_dwordx4 %0, %1, %2 offset:%3 sc1\n\ts_nop 1" :: "v"(voff), "v"(v), "s"(sbase), "i"(OFF) : "memory");
    else asm volatile("s_nop 4\n\tglobal_store_dwordx4 %0, %1, %2 offset:%3\n\ts_nop 1" :: "v"(voff), "v"(v), "s"(sbase), "i"(OFF) : "memory");
}
template <bool SC1 = false, class T> DI void gst_frag4(unsigned voff, void* sbase, T f0, T f1, T f2, T f3) {
    gst16<0, SC1>(voff, f0, sbase); gst16<1024, SC1>(voff, f1, sbase); gst16<2048, SC1>(voff, f2, sbase); gst16<3072, SC1>(voff, f3, sbase);
}
DI void arrive(unsigned* cnt) { __hip_atomic_fetch_add(cnt, 1u, __ATOMIC_RELAXED, __HIP_MEMORY_SCOPE_AGENT); }
DI void wait_count(unsigned* cnt, unsigned target) {
    for (unsigned spins = 0; spins < (1u << 20); ++spins) {
        if ((unsigned)__builtin_amdgcn_readfirstlane(__hip_atomic_load(cnt, __ATOMIC_RELAXED, __HIP_MEMORY_SCOPE_AGENT)) >= target) break;
        __builtin_amdgcn_s_sleep(2);
    }
    asm volatile("" ::: "memory");
}
DI int lane_fresh() { int l; asm volatile("v_mbcnt_lo_u32_b32 %0, -1, 0\n\tv_mbcnt_hi_u32_b32 %0, -1, %0" : "=v"(l)); return l; }
template <int N> DI void waitv() { asm volatile("s_waitcnt vmcnt(%0)" :: "i"(N) : "memory"); }
template <int N> DI void waitv4(bf16x8& a, bf16x8& b, bf16x8& c, bf16x8& d) {
    asm volatile("s_waitcnt vmcnt(%4)" : "+v"(a), "+v"(b), "+v"(c), "+v"(d) : "i"(N) : "memory");
}
DI void bar() { asm volatile("s_waitcnt lgkmcnt(0)\n\ts_barrier" ::: "memory"); }
#define SB() __builtin_amdgcn_sched_barrier(0)
#define PIN(x) asm volatile("" : "+v"(x))
DI void rawbar() { SB(); asm volatile("s_barrier" ::: "memory"); SB(); }
#define PDAV 4
constexpr int PDA = PDAV;
constexpr int PD = 3;
DI s16x4 trrd(LAS const char* p) { return __builtin_bit_cast(s16x4, __builtin_amdgcn_ds_read_tr16_b64_v4i16((LAS v4i16_t*)p)); }
DI bf16x8 trfrag(LAS const char* p, int off_lo, int off_hi) {
    s16x4 a = trrd(p + off_lo), b = trrd(p + off_hi);
    return (bf16x8){a[0], a[1], a[2], a[3], b[0], b[1], b[2], b[3]};
}
DI bf16x8 ldsfrag(LAS const char* p) { return *(LAS const bf16x8*)p; }
template <class T> DI T* opaque(T* p) { asm volatile("" : "+v"(p)); return p; }
DI bf16x8 pack8(const f32x16& v, int b, float s) {
    u32x4 w = {cvtpk(v[b] * s, v[b + 1] * s), cvtpk(v[b + 2] * s, v[b + 3] * s), cvtpk(v[b + 4] * s, v[b + 5] * s), cvtpk(v[b + 6] * s, v[b + 7] * s)};
    return __builtin_bit_cast(bf16x8, w);
}
DI bf16x8 pack8n(const f32x16& v, int b) {
    u32x4 w = {cvtpk(v[b], v[b + 1]), cvtpk(v[b + 2], v[b + 3]), cvtpk(v[b + 4], v[b + 5]), cvtpk(v[b + 6], v[b + 7])};
    return __builtin_bit_cast(bf16x8, w);
}
DI float xhalf_max(float v) {
    auto rr = __builtin_amdgcn_permlane32_swap(__float_as_uint(v), __float_as_uint(v), false, false);
    return fmaxf(__uint_as_float(rr[0]), __uint_as_float(rr[1]));
}
DI float xhalf_sum(float v) {
    auto rr = __builtin_amdgcn_permlane32_swap(__float_as_uint(v), __float_as_uint(v), false, false);
    return __uint_as_float(rr[0]) + __uint_as_float(rr[1]);
}

template <int CTRL> DI float dpp_add(float v) {
    return v + __uint_as_float(__builtin_amdgcn_update_dpp(0u, __float_as_uint(v), CTRL, 0xf, 0xf, true));
}
DI float wave_sum(float v) {
    v = dpp_add<0xB1>(v); v = dpp_add<0x4E>(v); v = dpp_add<0x141>(v); v = dpp_add<0x140>(v);
    auto r16 = __builtin_amdgcn_permlane16_swap(__float_as_uint(v), __float_as_uint(v), false, false);
    v = __uint_as_float(r16[0]) + __uint_as_float(r16[1]);
    return xhalf_sum(v);
}

__global__ __launch_bounds__(512, 2) void k_kv(const u16* __restrict__ x, const u16* __restrict__ ckv, const u16* __restrict__ wuk,
                                               const u16* __restrict__ wuv, u16* __restrict__ xn, u16* __restrict__ Kf, u16* __restrict__ Vf,
                                               unsigned* __restrict__ cnt) {
    __shared__ __attribute__((aligned(1024))) char lds_[163840];
    const int tid = threadIdx.x, lane = tid & 63, r32 = lane & 31, hi = lane >> 5;
    const int w = __builtin_amdgcn_readfirstlane(tid >> 6);
    const int b = blockIdx.x, xcd = b & 7, idx = b >> 3, h = xcd * 4 + (idx & 3), j = idx >> 2;
    const unsigned lds0 = (unsigned)(uintptr_t)lds_;
    LAS const char* lds = (LAS const char*)lds_;
    STAMP(0, 0);
    bf16x8 xrow;
    gld16<0>(xrow, (unsigned)(lane * 16), (const char*)x + (size_t)(b * 8 + w) * 1024);
    const unsigned wvoff = (unsigned)((8 * w + ((lane >> 2) & 7)) * 8192 + (lane >> 5) * 64 + (lane & 3) * 16);
    const unsigned avoff0 = (unsigned)((lane >> 3) * 1024 + (((lane & 7) ^ (lane >> 4)) * 16));
    const unsigned avoff1 = (unsigned)((lane >> 3) * 1024 + (((lane & 7) ^ (4 + (lane >> 4))) * 16));
    const char* abase = (const char*)ckv + (size_t)(j * 256 + w * 32) * 1024;
    const char* wkb = (const char*)wuk + h * 256;
    const char* wvb = (const char*)wuv + h * 256;
    const unsigned ldst = lds0 + w * 2048, adst = lds0 + 65536 + w * 12288;
    bf16x8 a[32];
    auto issue_p = [&](auto ic, auto pc) __attribute__((always_inline)) {
        constexpr int st = ic, p = pc;
        if constexpr (st < 16) {
            if constexpr (p < 2) glds16(wvoff, (st < 8 ? wkb : wvb) + (size_t)(st & 7) * 64 * 8192 + p * 128, ldst + (st & 3) * 16384 + p * 1024);
            else if constexpr (st < 8) glds16((p & 1) ? avoff1 : avoff0, abase + st * 128 + (p - 2) * 8192, adst + (st % 3) * 4096 + (p - 2) * 1024);
        }
    };
    sfor<0, 3>([&](auto ic) __attribute__((always_inline)) { sfor<0, 6>([&](auto pc) __attribute__((always_inline)) { issue_p(ic, pc); }); });
    const int g16 = lane >> 4, q4 = (lane >> 2) & 3, p4 = lane & 3;
    LAS const char* rb = lds + hi * 2048 + q4 * 64 + (g16 & 1) * 32 + p4 * 8;
    LAS const char* ra[4];
#pragma unroll
    for (int u = 0; u < 4; ++u) ra[u] = opaque(lds + 65536 + w * 12288 + r32 * 128 + (((2 * u + hi) ^ ((r32 >> 1) & 7)) * 16));
    f32x16 acc[4];
    const int rg = 8 * j + w;
    bf16x8 wc[PD];
    auto rdf = [&](int st, int i) __attribute__((always_inline)) {
        const int off = (st & 3) * 16384 + (i >> 2) * 4096 + (i & 3) * 512;
        return trfrag(rb, off, off + 256);
    };
    {
        asm volatile("s_waitcnt vmcnt(18)" : "+v"(xrow) :: "memory");
        u32x4 xu = __builtin_bit_cast(u32x4, xrow);
        float v[8];
#pragma unroll
        for (int e = 0; e < 4; ++e) { v[2 * e] = bf_lo(xu[e]); v[2 * e + 1] = bf_hi(xu[e]); }
        float s = 0.f;
#pragma unroll
        for (int e = 0; e < 8; ++e) s += v[e];
        s = wave_sum(s);
        const float mean = s * (1.0f / DM);
        float q = 0.f;
#pragma unroll
        for (int e = 0; e < 8; ++e) { const float d = v[e] - mean; q += d * d; }
        q = wave_sum(q);
        const float mb = rbf(mean), vb = rbf(q * (1.0f / DM));
        const float sd = rbf(__builtin_sqrtf(rbf(vb + 1e-5f))), rs = 1.0f / sd;
        u32x4 o;
#pragma unroll
        for (int e = 0; e < 4; ++e) o[e] = cvtpk(rbf(v[2 * e] - mb) * rs, rbf(v[2 * e + 1] - mb) * rs);
        const int row = b * 8 + w;
        *(u32x4*)((char*)xn + (size_t)(row >> 5) * 32768 + lane * 512 + (row & 31) * 16) = o;
    }
    if (b == 0 && tid < 48) cnt[tid * 64] = 0u;
    waitv<12>(); rawbar();
    STAMP(0, 1);
#pragma unroll
    for (int u = 0; u < 4; ++u) a[u] = ldsfrag(ra[u]);
#pragma unroll
    for (int i = 0; i < PD; ++i) wc[i] = rdf(0, i);
    sfor<0, 16>([&](auto ic) __attribute__((always_inline)) {
        constexpr int st = ic;
        constexpr int cnt = st <= 5 ? 6 : st <= 7 ? 2 : st <= 9 ? 10 : st <= 13 ? 2 : 0;
        waitv<cnt>();
        rawbar();
        bf16x8 wf[16 + PD];
#pragma unroll
        for (int i = 0; i < PD; ++i) wf[i] = wc[i];
        sfor<0, 16>([&](auto iic) __attribute__((always_inline)) {
            constexpr int i = iic;
            if constexpr (i + PD < 16) wf[i + PD] = rdf(st, i + PD);
            else if constexpr (st < 15) wf[i + PD] = rdf(st + 1, i + PD - 16);
            if constexpr (st < 7 && i >= 8 && i < 12) a[4 * (st + 1) + i - 8] = ldsfrag(ra[i - 8] + ((st + 1) % 3) * 4096);
            const f32x16 cin = ((st & 7) == 0 && i < 4) ? f32x16{} : acc[i & 3];
            if constexpr (st < 8) acc[i & 3] = MFMA(wf[i], a[4 * (st & 7) + (i >> 2)], cin);
            else acc[i & 3] = MFMA(a[4 * (st & 7) + (i >> 2)], wf[i], cin);
            if constexpr (i >= 1 && i < 7) issue_p(std::integral_constant<int, st + 3>{}, std::integral_constant<int, i - 1>{});
            SB();
        });
#pragma unroll
        for (int i = 0; i < PD; ++i) wc[i] = wf[16 + i];
        if constexpr (st == 7) STAMP(0, 2);
        if constexpr (st == 15) STAMP(0, 3);
        if constexpr (st == 7) {
            char* dst = (char*)Kf + (size_t)(h * 64 + rg) * 8192;
            gst_frag4((unsigned)(lane * 16), dst, pack8n(acc[0], 0), pack8n(acc[0], 8), pack8n(acc[1], 0), pack8n(acc[1], 8));
            gst_frag4((unsigned)(lane * 16), dst + 4096, pack8n(acc[2], 0), pack8n(acc[2], 8), pack8n(acc[3], 0), pack8n(acc[3], 8));
        }
        if constexpr (st == 15) {
            char* dst = (char*)Vf + (size_t)(h * 64 + rg) * 8192;
            gst_frag4((unsigned)(lane * 16), dst, pack8n(acc[0], 0), pack8n(acc[1], 0), pack8n(acc[2], 0), pack8n(acc[3], 0));
            gst_frag4((unsigned)(lane * 16), dst + 4096, pack8n(acc[0], 8), pack8n(acc[1], 8), pack8n(acc[2], 8), pack8n(acc[3], 8));
        }
    });
    STAMP(0, 4);
}

constexpr int A_K = 0, A_V = 65536, A_Q = 131072, A_LDS = 163840;

DI float max3f(float a, float b, float c) { return __builtin_fmaxf(__builtin_fmaxf(a, b), c); }
DI void cmask(f32x16& p0, f32x16& p1, int dq) {
    const float NEG = -__builtin_inff();
#pragma unroll
    for (int r = 0; r < 16; ++r) {
        const int c = (r & 3) + 8 * (r >> 2);
        if (c > dq) p0[r] = NEG;
        if (c + 32 > dq) p1[r] = NEG;
    }
}

__global__ __launch_bounds__(512, 2) void k_main(const u16* __restrict__ xn, const u16* __restrict__ wq, const u16* __restrict__ w1,
                                                 const u16* __restrict__ b1, const u16* __restrict__ Kf, const u16* __restrict__ Vf,
                                                 u16* Xf, const u16* __restrict__ wo, const u16* __restrict__ w2, float* P,
                                                 unsigned* cnt, const u16* __restrict__ x, const u16* __restrict__ b2, u16* __restrict__ out) {
    __shared__ __attribute__((aligned(1024))) char lds_[A_LDS];
    const int tid = threadIdx.x, lane = tid & 63, r32 = lane & 31, hi = lane >> 5;
    const int w = __builtin_amdgcn_readfirstlane(tid >> 6);
    const int b = blockIdx.x, xcd = b & 7, idx = b >> 3, h = xcd * 4 + (idx & 3), pi = idx >> 2;
    const int grp = w >> 2, wr = w & 3;
    const int qbig = 15 - pi, qsm = pi;
    const unsigned lds0 = (unsigned)(uintptr_t)lds_;
    LAS char* lds = (LAS char*)lds_;
    const int n1 = 2 * pi + 2;
    const int hl = idx & 3, hc0 = 224 * xcd + (hl < 3 ? 64 * hl : 160);
    STAMP(1, 0);
    const int rg_own = (grp == 0 ? qbig : qsm) * 4 + wr;
    const unsigned wvoff = (unsigned)((8 * w + ((lane >> 2) & 7)) * 8192 + (lane >> 5) * 64 + (lane & 3) * 16);
    const unsigned hvoff = (unsigned)((16 * w + ((lane >> 2) & 7)) * (FF * 2) + (lane >> 5) * 64 + (lane & 3) * 16);
    const unsigned tvoff = (unsigned)(lane * 16);
    const char* abase = (const char*)xn + (size_t)rg_own * 32768;
    const char* wqb = (const char*)wq + h * 256;
    const char* w1b = (const char*)w1 + hc0 * 2;
    const char* kgb = (const char*)Kf + (size_t)h * 32 * 16384;
    const char* vgb = (const char*)Vf + (size_t)h * 32 * 16384;
    const unsigned ldst = lds0 + w * 2048;
    auto issue_step = [&](const char* gb, unsigned region, int s, auto pc) __attribute__((always_inline)) {
        constexpr int p = pc;
        if (s <= 16 && (p == 0 || s >= n1)) {
            const int off = s < n1 ? s * 16384 + w * 2048 : (2 * s - n1) * 16384 + w * 4096;
            glds16x2<2048 * p>(tvoff, gb + off, lds0 + region + (unsigned)(off & 65535));
        }
    };
    constexpr std::integral_constant<int, 0> P0{}; constexpr std::integral_constant<int, 1> P1{};
    bf16x8 qf[8];
    {
        bf16x8 a[32];
        auto issue_p = [&](auto ic, auto pc) __attribute__((always_inline)) {
            constexpr int st = ic, p = pc;
            if constexpr (st < 8) {
                if constexpr (p < 2) glds16(wvoff, wqb + (size_t)st * 64 * 8192 + p * 128, ldst + A_V + (st & 3) * 16384 + p * 1024);
                else gld16<(p - 2) * 1024>(a[4 * st + p - 2], tvoff, abase + st * 4096);
            } else if constexpr (st < 12) {
                if constexpr (p < 2) glds16(hvoff, w1b + (size_t)((st - 8) * 128 + 8 * p) * (FF * 2), ldst + A_V + (st & 3) * 16384 + p * 1024);
                if constexpr (st == 11 && p == 2) glds4((unsigned)(r32 * 4), (const char*)b1 + hc0 * 2, lds0 + A_K + 3 * 16384 + w * 256);
            } else if constexpr (st < 14) {
                if constexpr (p == 0) issue_step(kgb, A_K, st - 12, P0);
            }
        };
        sfor<0, 3>([&](auto ic) __attribute__((always_inline)) { sfor<0, 6>([&](auto pc) __attribute__((always_inline)) { issue_p(ic, pc); }); });
        const int g16 = lane >> 4, q4 = (lane >> 2) & 3, p4 = lane & 3;
        LAS const char* rbq = opaque(lds + A_V + hi * 2048 + q4 * 64 + (g16 & 1) * 32 + p4 * 8);
        LAS const char* rbh = opaque(lds + A_V + hi * 1024 + q4 * 64 + (g16 & 1) * 32 + p4 * 8);
        auto rdf = [&](int st, int i) __attribute__((always_inline)) {
            if (st < 8) { const int off = (st & 3) * 16384 + (i >> 2) * 4096 + (i & 3) * 512; return trfrag(rbq, off, off + 256); }
            else { const int off = (st & 3) * 16384 + (i >> 1) * 2048 + (i & 1) * 512; return trfrag(rbh, off, off + 256); }
        };
        f32x16 acc[4];
        bf16x8 wc[PD];
        waitv<12>(); rawbar();
        STAMP(1, 1);
#pragma unroll
        for (int i = 0; i < PD; ++i) wc[i] = rdf(0, i);
        sfor<0, 12>([&](auto ic) __attribute__((always_inline)) {
            constexpr int st = ic;
            constexpr int cnt = st <= 5 ? 6 : st <= 8 ? 2 : st == 9 ? 3 : st == 10 ? 2 : 4;
            if constexpr (st < 8) waitv4<cnt>(a[4 * st], a[4 * st + 1], a[4 * st + 2], a[4 * st + 3]); else waitv<cnt>();
            rawbar();
            bf16x8 wf[16 + PD];
#pragma unroll
            for (int i = 0; i < PD; ++i) wf[i] = wc[i];
            sfor<0, 16>([&](auto iic) __attribute__((always_inline)) {
                constexpr int i = iic;
                if constexpr (i + PD < 16) wf[i + PD] = rdf(st, i + PD);
                else if constexpr (st < 11) wf[i + PD] = rdf(st + 1, i + PD - 16);
                if constexpr (st < 8) {
                    const f32x16 cin = (st == 0 && i < 4) ? f32x16{} : acc[i & 3];
                    acc[i & 3] = MFMA(wf[i], a[4 * st + (i >> 2)], cin);
                } else {
                    const f32x16 cin = (st == 8 && i < 2) ? f32x16{} : acc[i & 1];
                    acc[i & 1] = MFMA(wf[i], a[8 * (st - 8) + (i >> 1)], cin);
                }
                if constexpr (i >= 1 && i < 7) issue_p(std::integral_constant<int, st + 3>{}, std::integral_constant<int, i - 1>{});
                SB();
            });
#pragma unroll
            for (int i = 0; i < PD; ++i) wc[i] = wf[16 + i];
            if constexpr (st == 7) {
#pragma unroll
                for (int nt = 0; nt < 4; ++nt) { qf[2 * nt] = pack8(acc[nt], 0, QSCALE); qf[2 * nt + 1] = pack8(acc[nt], 8, QSCALE); }
            }
        });
#pragma unroll
        for (int nt = 0; nt < 2; ++nt)
#pragma unroll
            for (int g = 0; g < 4; ++g) {
                const u32x2 bb = *(LAS const u32x2*)(lds + A_K + 3 * 16384 + w * 256 + hi * 8 + (nt * 4 + g) * 16);
                const float bv[4] = {bf_lo(bb[0]), bf_hi(bb[0]), bf_lo(bb[1]), bf_hi(bb[1])};
#pragma unroll
                for (int e = 0; e < 4; ++e) { const float t = fmaxf(acc[nt][4 * g + e] + bv[e], 0.f); acc[nt][4 * g + e] = t * t; }
            }
        char* hdst = (char*)Xf + ((size_t)rg_own * KST + 256 + (hc0 >> 4)) * 1024;
        if (hl < 3) { gst16<0, true>(tvoff, pack8n(acc[0], 0), hdst); gst16<1024, true>(tvoff, pack8n(acc[0], 8), hdst); }
        gst16<2048, true>(tvoff, pack8n(acc[1], 0), hdst); gst16<3072, true>(tvoff, pack8n(acc[1], 8), hdst);
    }
    LAS const char* kb0 = lds + A_K + lane * 16;
    LAS const char* vb0 = opaque(lds + A_V + lane * 16);
    LAS char* qx = opaque(lds + A_Q + wr * 8192 + lane * 16);
    if (grp == 0) {
#pragma unroll
        for (int ks = 0; ks < 8; ++ks) *(LAS bf16x8*)(qx + ks * 1024) = qf[ks];
    }
    const int rg_big = qbig * 4 + wr;
    f32x16 o[4] = {f32x16{}, f32x16{}, f32x16{}, f32x16{}};
    f32x16 negm = f32x16{};
    float mh = 0.f, l = 0.f, il_sm = 0.f;
    int row0 = rg_own * 32;
    f32x16 sA0, sA1, sB0, sB1;
    u32x4 pw[4];
    bf16x8 kc[PDA], vc[PDA];
    bool pend = false; float fpend = 1.f;
    auto tile_of = [&](int s) __attribute__((always_inline)) { return s < n1 ? s : 2 * s - n1 + grp; };
    auto decide = [&](f32x16& c0, f32x16& c1, float rm, bool first) __attribute__((always_inline)) {
        rm = xhalf_max(rm);
        if (__builtin_expect(__any(rm > THR) || first, 0)) {
            const float dl = first ? __builtin_fmaxf(rm, -1e30f) : __builtin_fmaxf(rm, 0.f);
            mh += dl;
#pragma unroll
            for (int r = 0; r < 16; ++r) { c0[r] -= dl; c1[r] -= dl; negm[r] = -mh; }
            fpend = first ? 0.f : __builtin_amdgcn_exp2f(-dl);
            l *= fpend; pend = true;
        }
    };
    STAMP(1, 2);
    waitv<2>(); bar();
    issue_step(vgb, A_V, 0, P0);
    {
#pragma unroll
        for (int ks = 0; ks < 8; ++ks) {
            const bf16x8 k0 = ldsfrag(kb0 + ks * 1024), k1 = ldsfrag(kb0 + 8192 + ks * 1024);
            sA0 = MFMA(k0, qf[ks], ks == 0 ? f32x16{} : sA0);
            sA1 = MFMA(k1, qf[ks], ks == 0 ? f32x16{} : sA1);
        }
        waitv<2>(); bar();
        issue_step(kgb, A_K, 2, P0); issue_step(kgb, A_K, 2, P1);
        if (63 > row0) cmask(sA0, sA1, row0 + r32 - 4 * hi);
        float rm = max3f(sA0[0], sA0[1], sA1[0]);
        rm = __builtin_fmaxf(rm, sA1[1]);
#pragma unroll
        for (int r = 2; r < 16; ++r) rm = max3f(rm, sA0[r], sA1[r]);
        decide(sA0, sA1, rm, true);
        pend = false;
#pragma unroll
        for (int r = 0; r < 16; ++r) { sA0[r] = __builtin_amdgcn_exp2f(sA0[r]); sA1[r] = __builtin_amdgcn_exp2f(sA1[r]); }
#pragma unroll
        for (int i = 0; i < PDA; ++i) kc[i] = ldsfrag(kb0 + 16384 + (i & 1) * 8192 + (i >> 1) * 1024);
        waitv<0>();
        bar();
        if (tid == 0) arrive(cnt + xcd * 64);
    }
#define PH(k) do {} while (0)
    int tV, tK, tKd, tSl;
    {
        const int lf = lane_fresh(), s_ = lf & 31, up = lf >> 5;
        auto tbase = [&](int s) __attribute__((always_inline)) { const int sc = s < 16 ? s : 16, d = sc - n1; return (sc + (d > 0 ? d : 0)) << 14; };
        const int tv = tbase(s_), tk = tbase(s_ + 2), vtwo = s_ >= n1 ? 1 : 0, ktwo = s_ + 2 >= n1 ? 1 : 0;
        tV = tv + (w << (11 + vtwo)) + 2048 * (vtwo & up);
        tK = tk + (w << (11 + ktwo)) + 2048 * (ktwo & up);
        tKd = (int)(lds0 + A_K) + (((s_ + 2 <= 16 ? tk : tv) & 65535) + (w << (11 + ktwo))) + 2048 * (ktwo & up);
        tSl = (((tv >> 14) + (vtwo & grp)) & 3) << 14;
    }
    const unsigned spec = (1u << 16) | (grp ? (7u << (n1 - 2)) : 0u);
    const int nsw = grp ? n1 : -1;
    LAS const char* kb = kb0 + 16384;
    LAS const char* vb = vb0;
    LAS const char* kbn = kb0 + (tile_of(2) & 3) * 16384;
    auto step = [&](auto evn, f32x16& c0, f32x16& c1, f32x16& p0, f32x16& p1, const int s) __attribute__((always_inline)) {
        waitv<4>();
        rawbar();
        PH(0);
        {
            bf16x8 kf[16];
#pragma unroll
            for (int i = 0; i < PDA; ++i) kf[i] = kc[i];
            float sa = 0.f, sb = 0.f;
            unsigned dvo = 0, dm0 = 0;
            sfor<0, 16>([&](auto iic) __attribute__((always_inline)) {
                constexpr int i = iic;
                if constexpr (i + PDA < 16) kf[i + PDA] = ldsfrag(kb + ((i + PDA) & 1) * 8192 + ((i + PDA) >> 1) * 1024);
                else vc[i + PDA - 16] = ldsfrag(vb + (i + PDA - 16) * 1024);
                SB();
                if constexpr (i & 1) c1 = MFMA(kf[i], qf[i >> 1], i < 2 ? negm : c1);
                else c0 = MFMA(kf[i], qf[i >> 1], i < 2 ? negm : c0);
                const float e0 = i < 8 ? p0[2 * i] : p1[2 * i - 16], e1 = i < 8 ? p0[2 * i + 1] : p1[2 * i - 15];
                sa += e0; sb += e1;
                pw[i >> 2][i & 3] = cvtpk(e0, e1);
                PIN(sa); PIN(sb); PIN(pw[i >> 2]);
                if constexpr ((i & 7) == 1) { const int off = __builtin_amdgcn_readlane(tV, i < 8 ? s : s + 32);
                    dvo = tvoff + off; dm0 = lds0 + A_V + (off & 65535); PIN(dvo); }
                if constexpr ((i & 3) == 2) gldsl<1024 * ((i >> 2) & 1)>(dvo, vgb, dm0);
                SB();
            });
            l += sa + sb;
        }
        PH(1);
        waitv<4>();
        rawbar();
        PH(2);
        const bool sw = (bool)evn && s == nsw;
        const bool swn = !(bool)evn && s + 1 == nsw;
        {
            bf16x8 vf[16];
#pragma unroll
            for (int i = 0; i < PDA; ++i) vf[i] = vc[i];
            float ra = 0.f, rb2 = 0.f;
            unsigned dvo = 0, dm0 = 0;
            sfor<0, 16>([&](auto iic) __attribute__((always_inline)) {
                constexpr int i = iic;
                if constexpr (i + PDA < 16) vf[i + PDA] = ldsfrag(vb + (i + PDA) * 1024);
                else kc[i + PDA - 16] = ldsfrag(kbn + ((i + PDA - 16) & 1) * 8192 + ((i + PDA - 16) >> 1) * 1024);
                SB();
                o[i & 3] = MFMA(vf[i], __builtin_bit_cast(bf16x8, pw[i >> 2]), o[i & 3]);
                if constexpr (i == 0) {
                    if (__builtin_expect((spec >> s) & 1, 0)) {
                        if (sw) { il_sm = 1.0f / xhalf_sum(l); l = 0.f; }
                        const int lf = lane_fresh(); cmask(c0, c1, row0 + (lf & 31) - 64 * tile_of(s) - 4 * (lf >> 5));
                    }
                } else if constexpr (i < 5) {
                    constexpr int q = i - 1;
                    if constexpr (q == 0) { ra = max3f(c0[0], c0[1], c1[0]); rb2 = max3f(c0[2], c0[3], c1[1]); ra = max3f(ra, c1[2], c1[3]); }
                    else { ra = max3f(ra, c0[4 * q], c0[4 * q + 1]); rb2 = max3f(rb2, c0[4 * q + 2], c0[4 * q + 3]);
                           ra = max3f(ra, c1[4 * q], c1[4 * q + 1]); rb2 = max3f(rb2, c1[4 * q + 2], c1[4 * q + 3]); }
                    PIN(ra); PIN(rb2);
                } else if constexpr (i == 5) {
                    decide(c0, c1, __builtin_fmaxf(ra, rb2), sw);
                } else {
                    constexpr int e_lo = (32 * (i - 6)) / 10, e_hi = (32 * (i - 5)) / 10;
#pragma unroll
                    for (int e = e_lo; e < e_hi; ++e) {
                        if (e < 16) c0[e] = __builtin_amdgcn_exp2f(c0[e]); else c1[e - 16] = __builtin_amdgcn_exp2f(c1[e - 16]);
                    }
                    PIN(c0); PIN(c1);
                }
                if constexpr ((i & 7) == 1) { const int off = __builtin_amdgcn_readlane(tK, i < 8 ? s : s + 32);
                    dvo = tvoff + off; dm0 = (unsigned)__builtin_amdgcn_readlane(tKd, i < 8 ? s : s + 32); PIN(dvo); }
                if constexpr ((i & 3) == 2) gldsl<1024 * ((i >> 2) & 1)>(dvo, kgb, dm0);
                if constexpr (i == 15 - PDA) { vb = vb0 + __builtin_amdgcn_readlane(tSl, s); PIN(vb); }
                if constexpr (i == 15) { kb = kbn; kbn = kb0 + __builtin_amdgcn_readlane(tSl, s + 2); PIN(kb); PIN(kbn); }
                SB();
            });
        }
        PH(3);
        if (__builtin_expect(pend || swn, 0)) {
            if (sw) {
                char* dst = (char*)Xf + ((size_t)rg_own * KST + 8 * h) * 1024;
                gst_frag4<true>(tvoff, dst, pack8(o[0], 0, il_sm), pack8(o[0], 8, il_sm), pack8(o[1], 0, il_sm), pack8(o[1], 8, il_sm));
                gst_frag4<true>(tvoff, dst + 4096, pack8(o[2], 0, il_sm), pack8(o[2], 8, il_sm), pack8(o[3], 0, il_sm), pack8(o[3], 8, il_sm));
            }
            if (pend) {
#pragma unroll
                for (int d = 0; d < 4; ++d)
#pragma unroll
                    for (int r = 0; r < 16; ++r) o[d][r] *= fpend;
                pend = false;
            }
            if (swn) {
#pragma unroll
                for (int ks = 0; ks < 8; ++ks) qf[ks] = *(LAS const bf16x8*)(qx + ks * 1024);
                negm = f32x16{}; mh = 0.f; row0 = rg_big * 32;
            }
        }
        PH(4);
    };
    STAMP(1, 3);
    for (int s = 1; s < 17; s += 2) {
        if (s == 15 && w == 0) wait_count(cnt + xcd * 64, 32);
        step(std::false_type{}, sB0, sB1, sA0, sA1, s);
        step(std::true_type{}, sA0, sA1, sB0, sB1, s + 1);
    }
    STAMP(1, 4);
    {
        const int lane = lane_fresh(), hi = lane >> 5;
        const int xs = xcd, rb = idx >> 2, cb = idx & 3;
        const unsigned wvo = (unsigned)((8 * w + ((lane >> 2) & 7)) * 1024 + (lane >> 5) * 64 + (lane & 3) * 16);
        const unsigned wvo_h = (unsigned)((8 * (w & 3) + ((lane >> 2) & 7)) * 1024 + (lane >> 5) * 64 + (lane & 3) * 16);
        const int rg = rb * 8 + w;
        const unsigned avo = (unsigned)(lane * 16);
        const char* xa = (const char*)Xf + (size_t)rg * (KST * 1024) + (size_t)xs * 32 * 1024;
        const char* xh = (const char*)Xf + (size_t)rg * (KST * 1024) + (size_t)(256 + 14 * xs) * 1024;
        const char* wob = (const char*)wo + (size_t)xs * 512 * 1024 + cb * 256;
        const char* w2b = (const char*)w2 + (size_t)xs * 224 * 1024 + cb * 256;
        bf16x8 a[3][4];
        auto issueW = [&](auto ic) __attribute__((always_inline)) {
            constexpr int st = ic;
            const char* src = st < 4 ? w2b + (size_t)st * 65536 : wob + (size_t)(st - 4) * 65536;
            const unsigned vo = st == 3 ? wvo_h : wvo;
            glds16(vo, src, ldst + A_K + (st & 3) * 16384);
            glds16(vo, src + 128, ldst + A_K + (st & 3) * 16384 + 1024);
        };
        auto issueA = [&](auto ic) __attribute__((always_inline)) {
            constexpr int st = ic;
            const char* src = st < 4 ? xh + st * 4096 : xa + (st - 4) * 4096;
            gld16s<0>(a[st % 3][0], avo, src);
            gld16s<1024>(a[st % 3][1], avo, src);
            if constexpr (st != 3) { gld16s<2048>(a[st % 3][2], avo, src); gld16s<3072>(a[st % 3][3], avo, src); }
        };
        waitv<0>(); bar();
        issueW(std::integral_constant<int, 0>{}); issueA(std::integral_constant<int, 0>{});
        issueW(std::integral_constant<int, 1>{}); issueA(std::integral_constant<int, 1>{});
        issueW(std::integral_constant<int, 2>{});
        {
            float sa = 0.f;
#pragma unroll
            for (int r = 0; r < 16; ++r) sa += sA0[r] + sA1[r];
            l += sa;
            const bf16x8 pf[4] = {pack8n(sA0, 0), pack8n(sA0, 8), pack8n(sA1, 0), pack8n(sA1, 8)};
#pragma unroll
            for (int i = 0; i < 16; ++i) o[i & 3] = MFMA(ldsfrag(vb + i * 1024), pf[i >> 2], o[i & 3]);
        }
        bar();
        l = xhalf_sum(l);
        {
            LAS char* mine = opaque(lds + A_V + w * 8192 + lane * 16);
            LAS char* oth = opaque(lds + A_V + (w ^ 4) * 8192 + lane * 16);
            LAS float* ml = opaque((LAS float*)(lds + A_Q) + lane);
#pragma unroll
            for (int dd = 0; dd < 2; ++dd)
#pragma unroll
                for (int g = 0; g < 4; ++g) {
                    const f32x16& sv = grp == 0 ? o[2 + dd] : o[dd];
                    *(LAS f32x4*)(mine + (dd * 4 + g) * 1024) = (f32x4){sv[4 * g], sv[4 * g + 1], sv[4 * g + 2], sv[4 * g + 3]};
                }
            ml[w * 128] = mh; ml[w * 128 + 64] = l;
            bar();
            const float m2 = ml[(w ^ 4) * 128], l2 = ml[(w ^ 4) * 128 + 64];
            const float mn = fmaxf(mh, m2), f1 = __builtin_amdgcn_exp2f(mh - mn), f2 = __builtin_amdgcn_exp2f(m2 - mn);
            const float il = 1.0f / (l * f1 + l2 * f2), g1 = f1 * il, g2 = f2 * il;
            f32x16 sv[2];
#pragma unroll
            for (int dd = 0; dd < 2; ++dd) {
                sv[dd] = grp == 0 ? o[dd] : o[2 + dd];
#pragma unroll
                for (int g = 0; g < 4; ++g) {
                    const f32x4 t = *(LAS const f32x4*)(oth + (dd * 4 + g) * 1024);
#pragma unroll
                    for (int e = 0; e < 4; ++e) sv[dd][4 * g + e] = sv[dd][4 * g + e] * g1 + t[e] * g2;
                }
            }
            gst_frag4<true>(avo, (char*)Xf + ((size_t)rg_big * KST + 8 * h + 4 * grp) * 1024,
                            pack8n(sv[0], 0), pack8n(sv[0], 8), pack8n(sv[1], 0), pack8n(sv[1], 8));
        }
        STAMP(1, 5);
        waitv<0>(); bar();
        if (tid == 0) arrive(cnt + (8 + xcd) * 64);
        const int g16 = lane >> 4, q4 = (lane >> 2) & 3, p4 = lane & 3;
        LAS const char* rbp = opaque(lds + A_K + hi * 256 + q4 * 64 + 8 * (2 * (g16 & 1) + (p4 >> 1) + 4 * (p4 & 1)));
        f32x16 acc[4];
        bf16x8 wc[PD];
        auto rdf = [&](int st, int i) __attribute__((always_inline)) {
            const int off = (st & 3) * 16384 + (i >> 2) * 4096 + (i & 3) * 512;
            return trfrag(rbp, off, off + 2048);
        };
#pragma unroll
        for (int i = 0; i < PD; ++i) wc[i] = rdf(0, i);
        sfor<0, 12>([&](auto ic) __attribute__((always_inline)) {
            constexpr int st = ic;
            constexpr int cnt_ = st == 2 ? 4 : st <= 9 ? 6 : st == 10 ? 4 : 0;
            if constexpr (st == 2) { if (w == 0) wait_count(cnt + (8 + xcd) * 64, 32); }
            waitv4<cnt_>(a[st % 3][0], a[st % 3][1], a[st % 3][2], a[st % 3][3]);
            rawbar();
            if constexpr (st + 2 < 12) issueA(std::integral_constant<int, st + 2>{});
            if constexpr (st + 3 < 12) issueW(std::integral_constant<int, st + 3>{});
            SB();
            constexpr int NF = st == 3 ? 8 : 16;
            bf16x8 wf[16 + PD];
#pragma unroll
            for (int i = 0; i < PD; ++i) wf[i] = wc[i];
#pragma unroll
            for (int i = 0; i < NF; ++i) {
                if (i + PD < NF) wf[i + PD] = rdf(st, i + PD);
                else if (st < 11) wf[i + PD] = rdf(st + 1, i + PD - NF);
                const f32x16 cin = (st == 0 && i < 4) ? f32x16{} : acc[i & 3];
                acc[i & 3] = MFMA(wf[i], a[st % 3][i >> 2], cin);
                SB();
            }
#pragma unroll
            for (int i = 0; i < PD; ++i) wc[i] = wf[NF + i];
        });
        STAMP(2, 0);
        char* pdst = (char*)P + (((size_t)xs * 64 + rg) * 16 + cb * 4) * 2048;
        gst_frag4<true>(avo, pdst, pack8n(acc[0], 0), pack8n(acc[0], 8), pack8n(acc[1], 0), pack8n(acc[1], 8));
        gst_frag4<true>(avo, pdst + 4096, pack8n(acc[2], 0), pack8n(acc[2], 8), pack8n(acc[3], 0), pack8n(acc[3], 8));
        waitv<0>(); bar();
        if (tid == 0) arrive(cnt + (16 + idx) * 64);
        const int row = (rb * 8 + xs) * 32 + (lane & 31), col = cb * 128 + (w >> 1) * 32 + hi * 16 + 8 * (w & 1);
        u32x4 xv = *(const u32x4*)(x + (size_t)row * 512 + col), bv = *(const u32x4*)(b2 + col);
        PIN(xv); PIN(bv);
        if (w == 0) wait_count(cnt + (16 + idx) * 64, 8);
        bar();
        STAMP(2, 1);
        {
            const int rgr = rb * 8 + xs;
            const char* src = (const char*)P + ((size_t)rgr * 16 + cb * 4) * 2048 + w * 1024;
            u32x4 pv[8];
#pragma unroll
            for (int k = 0; k < 8; ++k) gld16s<0>(pv[k], avo, src + (size_t)k * (64 * 16 * 2048));
            asm volatile("s_waitcnt vmcnt(0)" : "+v"(pv[0]), "+v"(pv[1]), "+v"(pv[2]), "+v"(pv[3]), "+v"(pv[4]), "+v"(pv[5]), "+v"(pv[6]), "+v"(pv[7]) :: "memory");
            u32x4 o4;
#pragma unroll
            for (int e = 0; e < 4; ++e) {
                float lo = bf_lo(xv[e]) + bf_lo(bv[e]), hh = bf_hi(xv[e]) + bf_hi(bv[e]);
#pragma unroll
                for (int k = 0; k < 8; ++k) { lo += bf_lo(pv[k][e]); hh += bf_hi(pv[k][e]); }
                o4[e] = cvtpk(lo, hh);
            }
            *(u32x4*)(out + (size_t)row * 512 + col) = o4;
        }
        STAMP(2, 2); STAMP(2, 3);
    }
}

extern "C" void kernel_launch(void* const* d_in, const int* in_sizes, int n_in, void* d_out, int out_size, void* d_ws, size_t ws_size,
                              hipStream_t stream) {
    (void)in_sizes; (void)n_in; (void)out_size; (void)ws_size;
    const u16* x = (const u16*)d_in[0];
    const u16* ckv = (const u16*)d_in[1];
    const u16* wq = (const u16*)d_in[2];
    const u16* wuk = (const u16*)d_in[3];
    const u16* wuv = (const u16*)d_in[4];
    const u16* wo = (const u16*)d_in[5];
    const u16* w1 = (const u16*)d_in[7];
    const u16* b1 = (const u16*)d_in[8];
    const u16* w2 = (const u16*)d_in[9];
    const u16* b2 = (const u16*)d_in[10];
    char* ws = (char*)d_ws;
    u16* xn = (u16*)(ws + WS_XN);
    u16* Kf = (u16*)(ws + WS_K);
    u16* Vf = (u16*)(ws + WS_V);
    u16* Xf = (u16*)(ws + WS_X);
    float* P = (float*)(ws + WS_P);
    unsigned* cnt = (unsigned*)(ws + WS_C);
    k_kv<<<256, 512, 0, stream>>>(x, ckv, wuk, wuv, xn, Kf, Vf, cnt);
    k_main<<<256, 512, 0, stream>>>(xn, wq, w1, b1, Kf, Vf, Xf, wo, w2, P, cnt, x, b2, (u16*)d_out);
}
```

```cpp
#include <hip/hip_runtime.h>
#include <hip/hip_bf16.h>
#include <stdint.h>
#include <type_traits>

typedef unsigned short u16;
typedef short bf16x8 __attribute__((ext_vector_type(8)));
typedef short s16x4 __attribute__((ext_vector_type(4)));
typedef short v4i16_t __attribute__((ext_vector_type(4)));
typedef float f32x16 __attribute__((ext_vector_type(16)));
typedef float f32x4 __attribute__((ext_vector_type(4)));
typedef unsigned u32x4 __attribute__((ext_vector_type(4)));
typedef unsigned u32x2 __attribute__((ext_vector_type(2)));
#define LAS __attribute__((address_space(3)))
#define DI __device__ __forceinline__
#define MFMA(a, b, c) __builtin_amdgcn_mfma_f32_32x32x16_bf16(a, b, c, 0, 0, 0)

constexpr int SEQ = 2048, DM = 512, NH = 32, HD = 128, FF = 1792;
constexpr int KST = 368;
constexpr float QSCALE = 0.08838834764831845f * 1.4426950408889634f;
#define THRV 24.0f
constexpr float THR = THRV;

constexpr size_t WS_XN = 0;
constexpr size_t WS_K = 2u << 20;
constexpr size_t WS_V = WS_K + (16u << 20);
constexpr size_t WS_X = WS_V + (16u << 20);
constexpr size_t WS_P = WS_X + (size_t)64 * KST * 1024;
constexpr int NCNT = 80;
constexpr size_t WS_C = WS_P + ((size_t)32 << 20);
constexpr size_t WS_END = WS_C + NCNT * 256;

#define STAMP(k, i) do {} while (0)
template <int I, int N, class F> DI void sfor(F&& f) {
    if constexpr (I < N) { f(std::integral_constant<int, I>{}); sfor<I + 1, N>(f); }
}
typedef float f32x2 __attribute__((ext_vector_type(2)));
typedef __bf16 bf16x2_t __attribute__((ext_vector_type(2)));
DI unsigned cvtpk(float lo, float hi) { f32x2 v = {lo, hi}; return __builtin_bit_cast(unsigned, __builtin_convertvector(v, bf16x2_t)); }
DI float bf_lo(unsigned u) { return __uint_as_float(u << 16); }
DI float bf_hi(unsigned u) { return __uint_as_float(u & 0xffff0000u); }
DI float rbf(float f) { return bf_lo(cvtpk(f, f)); }
DI void glds16(unsigned voff, const void* sbase, unsigned lds_dst) {
    unsigned sv;
    asm volatile("s_mov_b32 %0, m0\n\ts_mov_b32 m0, %3\n\ts_nop 2\n\tglobal_load_lds_dwordx4 %1, %2\n\ts_mov_b32 m0, %0"
                 : "=&s"(sv) : "v"(voff), "s"(sbase), "s"(lds_dst) : "memory");
}
template <int OFF> DI void glds16x2(unsigned voff, const void* sbase, unsigned lds_dst) {
    unsigned sv;
    asm volatile("s_mov_b32 %0, m0\n\ts_mov_b32 m0, %3\n\ts_nop 2\n\tglobal_load_lds_dwordx4 %1, %2 offset:%4 sc1\n\tglobal_load_lds_dwordx4 %1, %2 offset:%5 sc1\n\ts_mov_b32 m0, %0"
                 : "=&s"(sv) : "v"(voff), "s"(sbase), "s"(lds_dst), "i"(OFF), "i"(OFF + 1024) : "memory");
}
template <int OFF> DI void glds16o(unsigned voff, const void* sbase, unsigned lds_dst) {
    unsigned sv;
    asm volatile("s_mov_b32 %0, m0\n\ts_mov_b32 m0, %3\n\ts_nop 2\n\tglobal_load_lds_dwordx4 %1, %2 offset:%4\n\ts_mov_b32 m0, %0"
                 : "=&s"(sv) : "v"(voff), "s"(sbase), "s"(lds_dst), "i"(OFF) : "memory");
}
template <int OFF> DI void glds16x4(unsigned voff, const void* sbase, unsigned lds_dst) {
    unsigned sv;
    asm volatile("s_mov_b32 %0, m0\n\ts_mov_b32 m0, %3\n\ts_nop 2\n\tglobal_load_lds_dwordx4 %1, %2 offset:%4\n\tglobal_load_lds_dwordx4 %1, %2 offset:%5\n\t"
                 "global_load_lds_dwordx4 %1, %2 offset:%6\n\tglobal_load_lds_dwordx4 %1, %2 offset:%7\n\ts_mov_b32 m0, %0"
                 : "=&s"(sv) : "v"(voff), "s"(sbase), "s"(lds_dst), "i"(OFF), "i"(OFF + 1024), "i"(OFF + 2048), "i"(OFF + 3072) : "memory");
}
template <int OFF> DI void gldsl(unsigned voff, const void* sbase, unsigned lds_dst) {
    asm volatile("s_mov_b32 m0, %2\n\ts_nop 0\n\tglobal_load_lds_dwordx4 %0, %1 offset:%3 sc1" :: "v"(voff), "s"(sbase), "s"(lds_dst), "i"(OFF) : "memory");
}
DI void glds4(unsigned voff, const void* sbase, unsigned lds_dst) {
    unsigned sv;
    asm volatile("s_mov_b32 %0, m0\n\ts_mov_b32 m0, %3\n\ts_nop 2\n\tglobal_load_lds_dword %1, %2\n\ts_mov_b32 m0, %0"
                 : "=&s"(sv) : "v"(voff), "s"(sbase), "s"(lds_dst) : "memory");
}
template <int OFF> DI void gld16(bf16x8& dst, unsigned voff, const void* sbase) {
    asm volatile("s_nop 4\n\tglobal_load_dwordx4 %0, %1, %2 offset:%3" : "=v"(dst) : "v"(voff), "s"(sbase), "i"(OFF) : "memory");
}
template <int OFF, class T> DI void gld16s(T& dst, unsigned voff, const void* sbase) {
    asm volatile("s_nop 4\n\tglobal_load_dwordx4 %0, %1, %2 offset:%3 sc1" : "=v"(dst) : "v"(voff), "s"(sbase), "i"(OFF) : "memory");
}
template <int OFF> DI void gld8(u32x2& dst, unsigned voff, const void* sbase) {
    asm volatile("s_nop 4\n\tglobal_load_dwordx2 %0, %1, %2 offset:%3" : "=v"(dst) : "v"(voff), "s"(sbase), "i"(OFF) : "memory");
}
template <int OFF, bool SC1 = false, class T> DI void gst16(unsigned voff, T v, void* sbase) {
    if constexpr (SC1) asm volatile("s_nop 4\n\tglobal_store_dwordx4 %0, %1, %2 offset:%3 sc1\n\ts_nop 1" :: "v"(voff), "v"(v), "s"(sbase), "i"(OFF) : "memory");
    else asm volatile("s_nop 4\n\tglobal_store_dwordx4 %0, %1, %2 offset:%3\n\ts_nop 1" :: "v"(voff), "v"(v), "s"(sbase), "i"(OFF) : "memory");
}
template <bool SC1 = false, class T> DI void gst_frag4(unsigned voff, void* sbase, T f0, T f1, T f2, T f3) {
    gst16<0, SC1>(voff, f0, sbase); gst16<1024, SC1>(voff, f1, sbase); gst16<2048, SC1>(voff, f2, sbase); gst16<3072, SC1>(voff, f3, sbase);
}
DI void arrive(unsigned* cnt) { __hip_atomic_fetch_add(cnt, 1u, __ATOMIC_RELAXED, __HIP_MEMORY_SCOPE_AGENT); }
DI void wait_count(unsigned* cnt, unsigned target) {
    for (unsigned spins = 0; spins < (1u << 20); ++spins) {
        if ((unsigned)__builtin_amdgcn_readfirstlane(__hip_atomic_load(cnt, __ATOMIC_RELAXED, __HIP_MEMORY_SCOPE_AGENT)) >= target) break;
        __builtin_amdgcn_s_sleep(2);
    }
    asm volatile("" ::: "memory");
}
DI int lane_fresh() { int l; asm volatile("v_mbcnt_lo_u32_b32 %0, -1, 0\n\tv_mbcnt_hi_u32_b32 %0, -1, %0" : "=v"(l)); return l; }
template <int N> DI void waitv() { asm volatile("s_waitcnt vmcnt(%0)" :: "i"(N) : "memory"); }
template <int N> DI void waitv4(bf16x8& a, bf16x8& b, bf16x8& c, bf16x8& d) {
    asm volatile("s_waitcnt vmcnt(%4)" : "+v"(a), "+v"(b), "+v"(c), "+v"(d) : "i"(N) : "memory");
}
DI void bar() { asm volatile("s_waitcnt lgkmcnt(0)\n\ts_barrier" ::: "memory"); }
#define SB() __builtin_amdgcn_sched_barrier(0)
#define PIN(x) asm volatile("" : "+v"(x))
DI void rawbar() { SB(); asm volatile("s_barrier" ::: "memory"); SB(); }
#define PDAV 4
constexpr int PDA = PDAV;
#define KVSC1 true
constexpr int PD = 3;
DI s16x4 trrd(LAS const char* p) { return __builtin_bit_cast(s16x4, __builtin_amdgcn_ds_read_tr16_b64_v4i16((LAS v4i16_t*)p)); }
DI bf16x8 trfrag(LAS const char* p, int off_lo, int off_hi) {
    s16x4 a = trrd(p + off_lo), b = trrd(p + off_hi);
    return (bf16x8){a[0], a[1], a[2], a[3], b[0], b[1], b[2], b[3]};
}
DI bf16x8 ldsfrag(LAS const char* p) { return *(LAS const bf16x8*)p; }
template <class T> DI T* opaque(T* p) { asm volatile("" : "+v"(p)); return p; }
DI bf16x8 pack8(const f32x16& v, int b, float s) {
    u32x4 w = {cvtpk(v[b] * s, v[b + 1] * s), cvtpk(v[b + 2] * s, v[b + 3] * s), cvtpk(v[b + 4] * s, v[b + 5] * s), cvtpk(v[b + 6] * s, v[b + 7] * s)};
    return __builtin_bit_cast(bf16x8, w);
}
DI bf16x8 pack8n(const f32x16& v, int b) {
    u32x4 w = {cvtpk(v[b], v[b + 1]), cvtpk(v[b + 2], v[b + 3]), cvtpk(v[b + 4], v[b + 5]), cvtpk(v[b + 6], v[b + 7])};
    return __builtin_bit_cast(bf16x8, w);
}
DI float xhalf_max(float v) {
    auto rr = __builtin_amdgcn_permlane32_swap(__float_as_uint(v), __float_as_uint(v), false, false);
    return fmaxf(__uint_as_float(rr[0]), __uint_as_float(rr[1]));
}
DI float xhalf_sum(float v) {
    auto rr = __builtin_amdgcn_permlane32_swap(__float_as_uint(v), __float_as_uint(v), false, false);
    return __uint_as_float(rr[0]) + __uint_as_float(rr[1]);
}

template <int CTRL> DI float dpp_add(float v) {
    return v + __uint_as_float(__builtin_amdgcn_update_dpp(0u, __float_as_uint(v), CTRL, 0xf, 0xf, true));
}
DI float wave_sum(float v) {
    v = dpp_add<0xB1>(v); v = dpp_add<0x4E>(v); v = dpp_add<0x141>(v); v = dpp_add<0x140>(v);
    auto r16 = __builtin_amdgcn_permlane16_swap(__float_as_uint(v), __float_as_uint(v), false, false);
    v = __uint_as_float(r16[0]) + __uint_as_float(r16[1]);
    return xhalf_sum(v);
}

__global__ __launch_bounds__(512) void k_pre(const u16* __restrict__ x, u16* __restrict__ xn, unsigned* __restrict__ cnt) {
    const int tid = threadIdx.x, lane = tid & 63, row = blockIdx.x * 8 + (tid >> 6);
    const u32x4 xu = *((const u32x4*)(x + (size_t)row * 512) + lane);
    float v[8];
#pragma unroll
    for (int e = 0; e < 4; ++e) { v[2 * e] = bf_lo(xu[e]); v[2 * e + 1] = bf_hi(xu[e]); }
    float s = 0.f;
#pragma unroll
    for (int e = 0; e < 8; ++e) s += v[e];
    s = wave_sum(s);
    const float mean = s * (1.0f / DM);
    float q = 0.f;
#pragma unroll
    for (int e = 0; e < 8; ++e) { const float d = v[e] - mean; q += d * d; }
    q = wave_sum(q);
    const float mb = rbf(mean), vb = rbf(q * (1.0f / DM));
    const float sd = rbf(__builtin_sqrtf(rbf(vb + 1e-5f))), rs = 1.0f / sd;
    u32x4 o;
#pragma unroll
    for (int e = 0; e < 4; ++e) o[e] = cvtpk(rbf(v[2 * e] - mb) * rs, rbf(v[2 * e + 1] - mb) * rs);
    *(u32x4*)((char*)xn + (size_t)(row >> 5) * 32768 + lane * 512 + (row & 31) * 16) = o;
    if (blockIdx.x == 0 && tid < NCNT) cnt[tid * 64] = 0u;
}

constexpr int A_K = 0, A_V = 65536, A_Q = 131072, A_LDS = 163840;

DI float max3f(float a, float b, float c) { return __builtin_fmaxf(__builtin_fmaxf(a, b), c); }
DI void cmask(f32x16& p0, f32x16& p1, int dq) {
    const float NEG = -__builtin_inff();
#pragma unroll
    for (int r = 0; r < 16; ++r) {
        const int c = (r & 3) + 8 * (r >> 2);
        if (c > dq) p0[r] = NEG;
        if (c + 32 > dq) p1[r] = NEG;
    }
}

__global__ __launch_bounds__(512, 2) void k_main(const u16* __restrict__ xn, const u16* __restrict__ wq, const u16* __restrict__ w1,
                                                 const u16* __restrict__ b1, u16* Kf, u16* Vf,
                                                 const u16* __restrict__ ckv, const u16* __restrict__ wuk, const u16* __restrict__ wuv,
                                                 u16* Xf, const u16* __restrict__ wo, const u16* __restrict__ w2, float* P,
                                                 unsigned* cnt, const u16* __restrict__ x, const u16* __restrict__ b2, u16* __restrict__ out) {
    __shared__ __attribute__((aligned(1024))) char lds_[A_LDS];
    const int tid = threadIdx.x, lane = tid & 63, r32 = lane & 31, hi = lane >> 5;
    const int w = __builtin_amdgcn_readfirstlane(tid >> 6);
    const int b = blockIdx.x, xcd = b & 7, idx = b >> 3, h = xcd * 4 + (idx & 3), pi = idx >> 2;
    const int grp = w >> 2, wr = w & 3;
    const int qbig = 15 - pi, qsm = pi;
    const unsigned lds0 = (unsigned)(uintptr_t)lds_;
    LAS char* lds = (LAS char*)lds_;
    const int n1 = 2 * pi + 2;
    const int hl = idx & 3, hc0 = 224 * xcd + (hl < 3 ? 64 * hl : 160);
    STAMP(1, 0);
    const int rg_own = (grp == 0 ? qbig : qsm) * 4 + wr;
    const unsigned wvoff = (unsigned)((8 * w + ((lane >> 2) & 7)) * 8192 + (lane >> 5) * 64 + (lane & 3) * 16);
    const unsigned hvoff = (unsigned)((16 * w + ((lane >> 2) & 7)) * (FF * 2) + (lane >> 5) * 64 + (lane & 3) * 16);
    const unsigned tvoff = (unsigned)(lane * 16);
    const char* abase = (const char*)xn + (size_t)rg_own * 32768;
    const char* wqb = (const char*)wq + h * 256;
    const char* w1b = (const char*)w1 + hc0 * 2;
    const char* kgb = (const char*)Kf + (size_t)h * 32 * 16384;
    const char* vgb = (const char*)Vf + (size_t)h * 32 * 16384;
    const unsigned ldst = lds0 + w * 2048;
    auto issue_step = [&](const char* gb, unsigned region, int s, auto pc) __attribute__((always_inline)) {
        constexpr int p = pc;
        if (s <= 16 && (p == 0 || s >= n1)) {
            const int off = s < n1 ? s * 16384 + w * 2048 : (2 * s - n1) * 16384 + w * 4096;
            glds16x2<2048 * p>(tvoff, gb + off, lds0 + region + (unsigned)(off & 65535));
        }
    };
    constexpr std::integral_constant<int, 0> P0{}; constexpr std::integral_constant<int, 1> P1{};
    bf16x8 qf[8];
    {
        bf16x8 a[32];
        const unsigned avoff0 = (unsigned)((lane >> 3) * 1024 + (((lane & 7) ^ (lane >> 4)) * 16));
        const unsigned avoff1 = (unsigned)((lane >> 3) * 1024 + (((lane & 7) ^ (4 + (lane >> 4))) * 16));
        const char* cbase = (const char*)ckv + (size_t)(pi * 256 + w * 32) * 1024;
        const char* wkb = (const char*)wuk + h * 256;
        const char* wvb = (const char*)wuv + h * 256;
        const unsigned adst = lds0 + w * 8192;
        auto issueW = [&](auto ic, auto pc) __attribute__((always_inline)) {
            constexpr int st = ic, p = pc;
            if constexpr (st < 24) glds16(wvoff, (st < 8 ? wkb : st < 16 ? wvb : wqb) + (size_t)(st & 7) * 64 * 8192 + p * 128, ldst + A_V + (st & 3) * 16384 + p * 1024);
            else if constexpr (st < 28) glds16(hvoff, w1b + (size_t)((st - 24) * 128 + 8 * p) * (FF * 2), ldst + A_V + (st & 3) * 16384 + p * 1024);
            else if constexpr (st < 30 && p == 0) issue_step(kgb, A_K, st - 28, P0);
        };
        auto issueC = [&](auto ic, auto ec) __attribute__((always_inline)) {
            constexpr int st = ic, e = ec;
            glds16((e & 1) ? avoff1 : avoff0, cbase + st * 128 + e * 8192, adst + (st & 1) * 4096 + e * 1024);
        };
        auto issue_p = [&](auto ic, auto pc) __attribute__((always_inline)) {
            constexpr int s_ = ic, p = pc;
            if constexpr (p < 2) issueW(std::integral_constant<int, s_ + 3>{}, pc);
            else if constexpr (s_ + 2 < 8) issueC(std::integral_constant<int, s_ + 2>{}, std::integral_constant<int, p - 2>{});
            else if constexpr (s_ >= 9 && s_ <= 16) gld16<(p - 2) * 1024>(a[4 * (s_ - 9) + p - 2], tvoff, abase + (s_ - 9) * 4096);
            else if constexpr (s_ == 24 && p == 2) glds4((unsigned)(r32 * 4), (const char*)b1 + hc0 * 2, lds0 + A_K + 3 * 16384 + w * 256);
        };
        sfor<0, 2>([&](auto ic) __attribute__((always_inline)) {
            issueW(ic, P0); issueW(ic, P1); sfor<0, 4>([&](auto ec) __attribute__((always_inline)) { issueC(ic, ec); }); });
        issueW(std::integral_constant<int, 2>{}, P0); issueW(std::integral_constant<int, 2>{}, P1);
        const int g16 = lane >> 4, q4 = (lane >> 2) & 3, p4 = lane & 3;
        LAS const char* rbq = opaque(lds + A_V + hi * 2048 + q4 * 64 + (g16 & 1) * 32 + p4 * 8);
        LAS const char* rbh = opaque(lds + A_V + hi * 1024 + q4 * 64 + (g16 & 1) * 32 + p4 * 8);
        LAS const char* ra[4];
#pragma unroll
        for (int u = 0; u < 4; ++u) ra[u] = lds + w * 8192 + r32 * 128 + (((2 * u + hi) ^ ((r32 >> 1) & 7)) * 16);
        auto rdf = [&](int st, int i) __attribute__((always_inline)) {
            if (st < 24) { const int off = (st & 3) * 16384 + (i >> 2) * 4096 + (i & 3) * 512; return trfrag(rbq, off, off + 256); }
            else { const int off = (st & 3) * 16384 + (i >> 1) * 2048 + (i & 1) * 512; return trfrag(rbh, off, off + 256); }
        };
        f32x16 acc[4];
        bf16x8 wc[PD];
        unsigned pollv = 0;
        waitv<8>(); rawbar();
        STAMP(1, 1);
#pragma unroll
        for (int u = 0; u < 4; ++u) a[u] = ldsfrag(ra[u]);
#pragma unroll
        for (int i = 0; i < PD; ++i) wc[i] = rdf(0, i);
        sfor<0, 28>([&](auto ic) __attribute__((always_inline)) {
            constexpr int st = ic;
            constexpr auto nops = [](int s_) { return (s_ + 3 < 30 ? 2 : 0) + ((s_ <= 5 || (s_ >= 9 && s_ <= 16)) ? 4 : 0) + (s_ == 24 ? 1 : 0); };
            constexpr int cnt_ = st == 0 ? 6 : nops(st - 1) + ((st - 1 == 7 || st - 1 == 15) ? 8 : 0);
            if constexpr (st == 24) {
                if (w == 0) { asm volatile("s_waitcnt vmcnt(%1)" : "+v"(pollv) : "i"(cnt_) : "memory");
                              if ((unsigned)__builtin_amdgcn_readfirstlane(pollv) < 8u) wait_count(cnt + (48 + h) * 64, 8); }
            }
            if constexpr (st >= 16 && st < 24) waitv4<cnt_>(a[4 * (st - 16)], a[4 * (st - 16) + 1], a[4 * (st - 16) + 2], a[4 * (st - 16) + 3]); else waitv<cnt_>();
            rawbar();
            if constexpr (st == 17) { if (tid == 0) arrive(cnt + (48 + h) * 64); }
            if constexpr (st == 21) { if (w == 0) asm volatile("s_nop 4\n\tglobal_load_dword %0, %1, %2 sc1" : "=v"(pollv) : "v"(0u), "s"(cnt + (48 + h) * 64) : "memory"); }
            bf16x8 wf[16 + PD];
#pragma unroll
            for (int i = 0; i < PD; ++i) wf[i] = wc[i];
            sfor<0, 16>([&](auto iic) __attribute__((always_inline)) {
                constexpr int i = iic;
                if constexpr (i + PD < 16) wf[i + PD] = rdf(st, i + PD);
                else if constexpr (st < 27) wf[i + PD] = rdf(st + 1, i + PD - 16);
                if constexpr (st < 7 && i == 11) waitv<st == 0 ? 8 : nops(st)>();
                if constexpr (st < 7 && i >= 12) a[4 * (st + 1) + i - 12] = ldsfrag(ra[i - 12] + ((st + 1) & 1) * 4096);
                if constexpr (st < 24) {
                    const f32x16 cin = ((st & 7) == 0 && i < 4) ? f32x16{} : acc[i & 3];
                    if constexpr (st >= 8 && st < 16) acc[i & 3] = MFMA(a[4 * (st & 7) + (i >> 2)], wf[i], cin);
                    else acc[i & 3] = MFMA(wf[i], a[4 * (st & 7) + (i >> 2)], cin);
                } else {
                    const f32x16 cin = (st == 24 && i < 2) ? f32x16{} : acc[i & 1];
                    acc[i & 1] = MFMA(wf[i], a[8 * (st - 24) + (i >> 1)], cin);
                }
                if constexpr (i >= 1 && i < 7) issue_p(ic, std::integral_constant<int, i - 1>{});
                SB();
            });
#pragma unroll
            for (int i = 0; i < PD; ++i) wc[i] = wf[16 + i];
            if constexpr (st == 7) {
                char* dst = (char*)Kf + (size_t)(h * 64 + pi * 8 + w) * 8192;
                gst_frag4<KVSC1>(tvoff, dst, pack8n(acc[0], 0), pack8n(acc[0], 8), pack8n(acc[1], 0), pack8n(acc[1], 8));
                gst_frag4<KVSC1>(tvoff, dst + 4096, pack8n(acc[2], 0), pack8n(acc[2], 8), pack8n(acc[3], 0), pack8n(acc[3], 8));
            }
            if constexpr (st == 15) {
                char* dst = (char*)Vf + (size_t)(h * 64 + pi * 8 + w) * 8192;
                gst_frag4<KVSC1>(tvoff, dst, pack8n(acc[0], 0), pack8n(acc[1], 0), pack8n(acc[2], 0), pack8n(acc[3], 0));
                gst_frag4<KVSC1>(tvoff, dst + 4096, pack8n(acc[0], 8), pack8n(acc[1], 8), pack8n(acc[2], 8), pack8n(acc[3], 8));
            }
            if constexpr (st == 23) {
#pragma unroll
                for (int nt = 0; nt < 4; ++nt) { qf[2 * nt] = pack8(acc[nt], 0, QSCALE); qf[2 * nt + 1] = pack8(acc[nt], 8, QSCALE); }
            }
        });
#pragma unroll
        for (int nt = 0; nt < 2; ++nt)
#pragma unroll
            for (int g = 0; g < 4; ++g) {
                const u32x2 bb = *(LAS const u32x2*)(lds + A_K + 3 * 16384 + w * 256 + hi * 8 + (nt * 4 + g) * 16);
                const float bv[4] = {bf_lo(bb[0]), bf_hi(bb[0]), bf_lo(bb[1]), bf_hi(bb[1])};
#pragma unroll
                for (int e = 0; e < 4; ++e) { const float t = fmaxf(acc[nt][4 * g + e] + bv[e], 0.f); acc[nt][4 * g + e] = t * t; }
            }
        char* hdst = (char*)Xf + ((size_t)rg_own * KST + 256 + (hc0 >> 4)) * 1024;
        if (hl < 3) { gst16<0, true>(tvoff, pack8n(acc[0], 0), hdst); gst16<1024, true>(tvoff, pack8n(acc[0], 8), hdst); }
        gst16<2048, true>(tvoff, pack8n(acc[1], 0), hdst); gst16<3072, true>(tvoff, pack8n(acc[1], 8), hdst);
    }
    LAS const char* kb0 = lds + A_K + lane * 16;
    LAS const char* vb0 = opaque(lds + A_V + lane * 16);
    LAS char* qx = opaque(lds + A_Q + wr * 8192 + lane * 16);
    if (grp == 0) {
#pragma unroll
        for (int ks = 0; ks < 8; ++ks) *(LAS bf16x8*)(qx + ks * 1024) = qf[ks];
    }
    const int rg_big = qbig * 4 + wr;
    f32x16 o[4] = {f32x16{}, f32x16{}, f32x16{}, f32x16{}};
    f32x16 negm = f32x16{};
    float mh = 0.f, l = 0.f, il_sm = 0.f;
    int row0 = rg_own * 32;
    f32x16 sA0, sA1, sB0, sB1;
    u32x4 pw[4];
    bf16x8 kc[PDA], vc[PDA];
    bool pend = false; float fpend = 1.f;
    auto tile_of = [&](int s) __attribute__((always_inline)) { return s < n1 ? s : 2 * s - n1 + grp; };
    auto decide = [&](f32x16& c0, f32x16& c1, float rm, bool first) __attribute__((always_inline)) {
        rm = xhalf_max(rm);
        if (__builtin_expect(__any(rm > THR) || first, 0)) {
            const float dl = first ? __builtin_fmaxf(rm, -1e30f) : __builtin_fmaxf(rm, 0.f);
            mh += dl;
#pragma unroll
            for (int r = 0; r < 16; ++r) { c0[r] -= dl; c1[r] -= dl; negm[r] = -mh; }
            fpend = first ? 0.f : __builtin_amdgcn_exp2f(-dl);
            l *= fpend; pend = true;
        }
    };
    STAMP(1, 2);
    waitv<2>(); bar();
    issue_step(vgb, A_V, 0, P0);
    {
#pragma unroll
        for (int ks = 0; ks < 8; ++ks) {
            const bf16x8 k0 = ldsfrag(kb0 + ks * 1024), k1 = ldsfrag(kb0 + 8192 + ks * 1024);
            sA0 = MFMA(k0, qf[ks], ks == 0 ? f32x16{} : sA0);
            sA1 = MFMA(k1, qf[ks], ks == 0 ? f32x16{} : sA1);
        }
        waitv<2>(); bar();
        issue_step(kgb, A_K, 2, P0); issue_step(kgb, A_K, 2, P1);
        if (63 > row0) cmask(sA0, sA1, row0 + r32 - 4 * hi);
        float rm = max3f(sA0[0], sA0[1], sA1[0]);
        rm = __builtin_fmaxf(rm, sA1[1]);
#pragma unroll
        for (int r = 2; r < 16; ++r) rm = max3f(rm, sA0[r], sA1[r]);
        decide(sA0, sA1, rm, true);
        pend = false;
#pragma unroll
        for (int r = 0; r < 16; ++r) { sA0[r] = __builtin_amdgcn_exp2f(sA0[r]); sA1[r] = __builtin_amdgcn_exp2f(sA1[r]); }
#pragma unroll
        for (int i = 0; i < PDA; ++i) kc[i] = ldsfrag(kb0 + 16384 + (i & 1) * 8192 + (i >> 1) * 1024);
        waitv<0>();
        bar();
        if (tid == 0) arrive(cnt + xcd * 64);
    }
#define PH(k) do {} while (0)
    int tV, tK, tKd, tSl;
    {
        const int lf = lane_fresh(), s_ = lf & 31, up = lf >> 5;
        auto tbase = [&](int s) __attribute__((always_inline)) { const int sc = s < 16 ? s : 16, d = sc - n1; return (sc + (d > 0 ? d : 0)) << 14; };
        const int tv = tbase(s_), tk = tbase(s_ + 2), vtwo = s_ >= n1 ? 1 : 0, ktwo = s_ + 2 >= n1 ? 1 : 0;
        tV = tv + (w << (11 + vtwo)) + 2048 * (vtwo & up);
        tK = tk + (w << (11 + ktwo)) + 2048 * (ktwo & up);
        tKd = (int)(lds0 + A_K) + (((s_ + 2 <= 16 ? tk : tv) & 65535) + (w << (11 + ktwo))) + 2048 * (ktwo & up);
        tSl = (((tv >> 14) + (vtwo & grp)) & 3) << 14;
    }
    const unsigned spec = (1u << 16) | (grp ? (7u << (n1 - 2)) : 0u);
    const int nsw = grp ? n1 : -1;
    LAS const char* kb = kb0 + 16384;
    LAS const char* vb = vb0;
    LAS const char* kbn = kb0 + (tile_of(2) & 3) * 16384;
    auto step = [&](auto evn, f32x16& c0, f32x16& c1, f32x16& p0, f32x16& p1, const int s) __attribute__((always_inline)) {
        waitv<4>();
        rawbar();
        PH(0);
        {
            bf16x8 kf[16];
#pragma unroll
            for (int i = 0; i < PDA; ++i) kf[i] = kc[i];
            float sa = 0.f, sb = 0.f;
            unsigned dvo = 0, dm0 = 0;
            sfor<0, 16>([&](auto iic) __attribute__((always_inline)) {
                constexpr int i = iic;
                if constexpr (i + PDA < 16) kf[i + PDA] = ldsfrag(kb + ((i + PDA) & 1) * 8192 + ((i + PDA) >> 1) * 1024);
                else vc[i + PDA - 16] = ldsfrag(vb + (i + PDA - 16) * 1024);
                SB();
                if constexpr (i & 1) c1 = MFMA(kf[i], qf[i >> 1], i < 2 ? negm : c1);
                else c0 = MFMA(kf[i], qf[i >> 1], i < 2 ? negm : c0);
                const float e0 = i < 8 ? p0[2 * i] : p1[2 * i - 16], e1 = i < 8 ? p0[2 * i + 1] : p1[2 * i - 15];
                sa += e0; sb += e1;
                pw[i >> 2][i & 3] = cvtpk(e0, e1);
                PIN(sa); PIN(sb); PIN(pw[i >> 2]);
                if constexpr ((i & 7) == 1) { const int off = __builtin_amdgcn_readlane(tV, i < 8 ? s : s + 32);
                    dvo = tvoff + off; dm0 = lds0 + A_V + (off & 65535); PIN(dvo); }
                if constexpr ((i & 3) == 2) gldsl<1024 * ((i >> 2) & 1)>(dvo, vgb, dm0);
                SB();
            });
            l += sa + sb;
        }
        PH(1);
        waitv<4>();
        rawbar();
        PH(2);
        const bool sw = (bool)evn && s == nsw;
        const bool swn = !(bool)evn && s + 1 == nsw;
        {
            bf16x8 vf[16];
#pragma unroll
            for (int i = 0; i < PDA; ++i) vf[i] = vc[i];
            float ra = 0.f, rb2 = 0.f;
            unsigned dvo = 0, dm0 = 0;
            sfor<0, 16>([&](auto iic) __attribute__((always_inline)) {
                constexpr int i = iic;
                if constexpr (i + PDA < 16) vf[i + PDA] = ldsfrag(vb + (i + PDA) * 1024);
                else kc[i + PDA - 16] = ldsfrag(kbn + ((i + PDA - 16) & 1) * 8192 + ((i + PDA - 16) >> 1) * 1024);
                SB();
                o[i & 3] = MFMA(vf[i], __builtin_bit_cast(bf16x8, pw[i >> 2]), o[i & 3]);
                if constexpr (i == 0) {
                    if (__builtin_expect((spec >> s) & 1, 0)) {
                        if (sw) { il_sm = 1.0f / xhalf_sum(l); l = 0.f; }
                        const int lf = lane_fresh(); cmask(c0, c1, row0 + (lf & 31) - 64 * tile_of(s) - 4 * (lf >> 5));
                    }
                } else if constexpr (i < 5) {
                    constexpr int q = i - 1;
                    if constexpr (q == 0) { ra = max3f(c0[0], c0[1], c1[0]); rb2 = max3f(c0[2], c0[3], c1[1]); ra = max3f(ra, c1[2], c1[3]); }
                    else { ra = max3f(ra, c0[4 * q], c0[4 * q + 1]); rb2 = max3f(rb2, c0[4 * q + 2], c0[4 * q + 3]);
                           ra = max3f(ra, c1[4 * q], c1[4 * q + 1]); rb2 = max3f(rb2, c1[4 * q + 2], c1[4 * q + 3]); }
                    PIN(ra); PIN(rb2);
                } else if constexpr (i == 5) {
                    decide(c0, c1, __builtin_fmaxf(ra, rb2), sw);
                } else {
                    constexpr int e_lo = (32 * (i - 6)) / 10, e_hi = (32 * (i - 5)) / 10;
#pragma unroll
                    for (int e = e_lo; e < e_hi; ++e) {
                        if (e < 16) c0[e] = __builtin_amdgcn_exp2f(c0[e]); else c1[e - 16] = __builtin_amdgcn_exp2f(c1[e - 16]);
                    }
                    PIN(c0); PIN(c1);
                }
                if constexpr ((i & 7) == 1) { const int off = __builtin_amdgcn_readlane(tK, i < 8 ? s : s + 32);
                    dvo = tvoff + off; dm0 = (unsigned)__builtin_amdgcn_readlane(tKd, i < 8 ? s : s + 32); PIN(dvo); }
                if constexpr ((i & 3) == 2) gldsl<1024 * ((i >> 2) & 1)>(dvo, kgb, dm0);
                if constexpr (i == 15 - PDA) { vb = vb0 + __builtin_amdgcn_readlane(tSl, s); PIN(vb); }
                if constexpr (i == 15) { kb = kbn; kbn = kb0 + __builtin_amdgcn_readlane(tSl, s + 2); PIN(kb); PIN(kbn); }
                SB();
            });
        }
        PH(3);
        if (__builtin_expect(pend || swn, 0)) {
            if (sw) {
                char* dst = (char*)Xf + ((size_t)rg_own * KST + 8 * h) * 1024;
                gst_frag4<true>(tvoff, dst, pack8(o[0], 0, il_sm), pack8(o[0], 8, il_sm), pack8(o[1], 0, il_sm), pack8(o[1], 8, il_sm));
                gst_frag4<true>(tvoff, dst + 4096, pack8(o[2], 0, il_sm), pack8(o[2], 8, il_sm), pack8(o[3], 0, il_sm), pack8(o[3], 8, il_sm));
            }
            if (pend) {
#pragma unroll
                for (int d = 0; d < 4; ++d)
#pragma unroll
                    for (int r = 0; r < 16; ++r) o[d][r] *= fpend;
                pend = false;
            }
            if (swn) {
#pragma unroll
                for (int ks = 0; ks < 8; ++ks) qf[ks] = *(LAS const bf16x8*)(qx + ks * 1024);
                negm = f32x16{}; mh = 0.f; row0 = rg_big * 32;
            }
        }
        PH(4);
    };
    STAMP(1, 3);
    for (int s = 1; s < 17; s += 2) {
        if (s == 15 && w == 0) wait_count(cnt + xcd * 64, 32);
        step(std::false_type{}, sB0, sB1, sA0, sA1, s);
        step(std::true_type{}, sA0, sA1, sB0, sB1, s + 1);
    }
    STAMP(1, 4);
    {
        const int lane = lane_fresh(), hi = lane >> 5;
        const int xs = xcd, rb = idx >> 2, cb = idx & 3;
        const unsigned wvo = (unsigned)((8 * w + ((lane >> 2) & 7)) * 1024 + (lane >> 5) * 64 + (lane & 3) * 16);
        const unsigned wvo_h = (unsigned)((8 * (w & 3) + ((lane >> 2) & 7)) * 1024 + (lane >> 5) * 64 + (lane & 3) * 16);
        const int rg = rb * 8 + w;
        const unsigned avo = (unsigned)(lane * 16);
        const char* xa = (const char*)Xf + (size_t)rg * (KST * 1024) + (size_t)xs * 32 * 1024;
        const char* xh = (const char*)Xf + (size_t)rg * (KST * 1024) + (size_t)(256 + 14 * xs) * 1024;
        const char* wob = (const char*)wo + (size_t)xs * 512 * 1024 + cb * 256;
        const char* w2b = (const char*)w2 + (size_t)xs * 224 * 1024 + cb * 256;
        bf16x8 a[3][4];
        auto issueW = [&](auto ic) __attribute__((always_inline)) {
            constexpr int st = ic;
            const char* src = st < 4 ? w2b + (size_t)st * 65536 : wob + (size_t)(st - 4) * 65536;
            const unsigned vo = st == 3 ? wvo_h : wvo;
            glds16(vo, src, ldst + A_K + (st & 3) * 16384);
            glds16(vo, src + 128, ldst + A_K + (st & 3) * 16384 + 1024);
        };
        auto issueA = [&](auto ic) __attribute__((always_inline)) {
            constexpr int st = ic;
            const char* src = st < 4 ? xh + st * 4096 : xa + (st - 4) * 4096;
            gld16s<0>(a[st % 3][0], avo, src);
            gld16s<1024>(a[st % 3][1], avo, src);
            if constexpr (st != 3) { gld16s<2048>(a[st % 3][2], avo, src); gld16s<3072>(a[st % 3][3], avo, src); }
        };
        waitv<0>(); bar();
        issueW(std::integral_constant<int, 0>{}); issueA(std::integral_constant<int, 0>{});
        issueW(std::integral_constant<int, 1>{}); issueA(std::integral_constant<int, 1>{});
        issueW(std::integral_constant<int, 2>{});
        {
            float sa = 0.f;
#pragma unroll
            for (int r = 0; r < 16; ++r) sa += sA0[r] + sA1[r];
            l += sa;
            const bf16x8 pf[4] = {pack8n(sA0, 0), pack8n(sA0, 8), pack8n(sA1, 0), pack8n(sA1, 8)};
#pragma unroll
            for (int i = 0; i < 16; ++i) o[i & 3] = MFMA(ldsfrag(vb + i * 1024), pf[i >> 2], o[i & 3]);
        }
        bar();
        l = xhalf_sum(l);
        {
            LAS char* mine = opaque(lds + A_V + w * 8192 + lane * 16);
            LAS char* oth = opaque(lds + A_V + (w ^ 4) * 8192 + lane * 16);
            LAS float* ml = opaque((LAS float*)(lds + A_Q) + lane);
#pragma unroll
            for (int dd = 0; dd < 2; ++dd)
#pragma unroll
                for (int g = 0; g < 4; ++g) {
                    const f32x16& sv = grp == 0 ? o[2 + dd] : o[dd];
                    *(LAS f32x4*)(mine + (dd * 4 + g) * 1024) = (f32x4){sv[4 * g], sv[4 * g + 1], sv[4 * g + 2], sv[4 * g + 3]};
                }
            ml[w * 128] = mh; ml[w * 128 + 64] = l;
            bar();
            const float m2 = ml[(w ^ 4) * 128], l2 = ml[(w ^ 4) * 128 + 64];
            const float mn = fmaxf(mh, m2), f1 = __builtin_amdgcn_exp2f(mh - mn), f2 = __builtin_amdgcn_exp2f(m2 - mn);
            const float il = 1.0f / (l * f1 + l2 * f2), g1 = f1 * il, g2 = f2 * il;
            f32x16 sv[2];
#pragma unroll
            for (int dd = 0; dd < 2; ++dd) {
                sv[dd] = grp == 0 ? o[dd] : o[2 + dd];
#pragma unroll
                for (int g = 0; g < 4; ++g) {
                    const f32x4 t = *(LAS const f32x4*)(oth + (dd * 4 + g) * 1024);
#pragma unroll
                    for (int e = 0; e < 4; ++e) sv[dd][4 * g + e] = sv[dd][4 * g + e] * g1 + t[e] * g2;
                }
            }
            gst_frag4<true>(avo, (char*)Xf + ((size_t)rg_big * KST + 8 * h + 4 * grp) * 1024,
                            pack8n(sv[0], 0), pack8n(sv[0], 8), pack8n(sv[1], 0), pack8n(sv[1], 8));
        }
        STAMP(1, 5);
        waitv<0>(); bar();
        if (tid == 0) arrive(cnt + (8 + xcd) * 64);
        const int g16 = lane >> 4, q4 = (lane >> 2) & 3, p4 = lane & 3;
        LAS const char* rbp = opaque(lds + A_K + hi * 256 + q4 * 64 + 8 * (2 * (g16 & 1) + (p4 >> 1) + 4 * (p4 & 1)));
        f32x16 acc[4];
        bf16x8 wc[PD];
        auto rdf = [&](int st, int i) __attribute__((always_inline)) {
            const int off = (st & 3) * 16384 + (i >> 2) * 4096 + (i & 3) * 512;
            return trfrag(rbp, off, off + 2048);
        };
#pragma unroll
        for (int i = 0; i < PD; ++i) wc[i] = rdf(0, i);
        sfor<0, 12>([&](auto ic) __attribute__((always_inline)) {
            constexpr int st = ic;
            constexpr int cnt_ = st == 2 ? 4 : st <= 9 ? 6 : st == 10 ? 4 : 0;
            if constexpr (st == 2) { if (w == 0) wait_count(cnt + (8 + xcd) * 64, 32); }
            waitv4<cnt_>(a[st % 3][0], a[st % 3][1], a[st % 3][2], a[st % 3][3]);
            rawbar();
            if constexpr (st + 2 < 12) issueA(std::integral_constant<int, st + 2>{});
            if constexpr (st + 3 < 12) issueW(std::integral_constant<int, st + 3>{});
            SB();
            constexpr int NF = st == 3 ? 8 : 16;
            bf16x8 wf[16 + PD];
#pragma unroll
            for (int i = 0; i < PD; ++i) wf[i] = wc[i];
#pragma unroll
            for (int i = 0; i < NF; ++i) {
                if (i + PD < NF) wf[i + PD] = rdf(st, i + PD);
                else if (st < 11) wf[i + PD] = rdf(st + 1, i + PD - NF);
                const f32x16 cin = (st == 0 && i < 4) ? f32x16{} : acc[i & 3];
                acc[i & 3] = MFMA(wf[i], a[st % 3][i >> 2], cin);
                SB();
            }
#pragma unroll
            for (int i = 0; i < PD; ++i) wc[i] = wf[NF + i];
        });
        STAMP(2, 0);
        char* pdst = (char*)P + (((size_t)xs * 64 + rg) * 16 + cb * 4) * 2048;
        gst_frag4<true>(avo, pdst, pack8n(acc[0], 0), pack8n(acc[0], 8), pack8n(acc[1], 0), pack8n(acc[1], 8));
        gst_frag4<true>(avo, pdst + 4096, pack8n(acc[2], 0), pack8n(acc[2], 8), pack8n(acc[3], 0), pack8n(acc[3], 8));
        waitv<0>(); bar();
        if (tid == 0) arrive(cnt + (16 + idx) * 64);
        const int row = (rb * 8 + xs) * 32 + (lane & 31), col = cb * 128 + (w >> 1) * 32 + hi * 16 + 8 * (w & 1);
        u32x4 xv = *(const u32x4*)(x + (size_t)row * 512 + col), bv = *(const u32x4*)(b2 + col);
        PIN(xv); PIN(bv);
        if (w == 0) wait_count(cnt + (16 + idx) * 64, 8);
        bar();
        STAMP(2, 1);
        {
            const int rgr = rb * 8 + xs;
            const char* src = (const char*)P + ((size_t)rgr * 16 + cb * 4) * 2048 + w * 1024;
            u32x4 pv[8];
#pragma unroll
            for (int k = 0; k < 8; ++k) gld16s<0>(pv[k], avo, src + (size_t)k * (64 * 16 * 2048));
            asm volatile("s_waitcnt vmcnt(0)" : "+v"(pv[0]), "+v"(pv[1]), "+v"(pv[2]), "+v"(pv[3]), "+v"(pv[4]), "+v"(pv[5]), "+v"(pv[6]), "+v"(pv[7]) :: "memory");
            u32x4 o4;
#pragma unroll
            for (int e = 0; e < 4; ++e) {
                float lo = bf_lo(xv[e]) + bf_lo(bv[e]), hh = bf_hi(xv[e]) + bf_hi(bv[e]);
#pragma unroll
                for (int k = 0; k < 8; ++k) { lo += bf_lo(pv[k][e]); hh += bf_hi(pv[k][e]); }
                o4[e] = cvtpk(lo, hh);
            }
            *(u32x4*)(out + (size_t)row * 512 + col) = o4;
        }
        STAMP(2, 2); STAMP(2, 3);
    }
}

extern "C" void kernel_launch(void* const* d_in, const int* in_sizes, int n_in, void* d_out, int out_size, void* d_ws, size_t ws_size,
                              hipStream_t stream) {
    (void)in_sizes; (void)n_in; (void)out_size; (void)ws_size;
    const u16* x = (const u16*)d_in[0];
    const u16* ckv = (const u16*)d_in[1];
    const u16* wq = (const u16*)d_in[2];
    const u16* wuk = (const u16*)d_in[3];
    const u16* wuv = (const u16*)d_in[4];
    const u16* wo = (const u16*)d_in[5];
    const u16* w1 = (const u16*)d_in[7];
    const u16* b1 = (const u16*)d_in[8];
    const u16* w2 = (const u16*)d_in[9];
    const u16* b2 = (const u16*)d_in[10];
    char* ws = (char*)d_ws;
    u16* xn = (u16*)(ws + WS_XN);
    u16* Kf = (u16*)(ws + WS_K);
    u16* Vf = (u16*)(ws + WS_V);
    u16* Xf = (u16*)(ws + WS_X);
    float* P = (float*)(ws + WS_P);
    unsigned* cnt = (unsigned*)(ws + WS_C);
    k_pre<<<256, 512, 0, stream>>>(x, xn, cnt);
    k_main<<<256, 512, 0, stream>>>(xn, wq, w1, b1, Kf, Vf, ckv, wuk, wuv, Xf, wo, w2, P, cnt, x, b2, (u16*)d_out);
}
```

```cpp
#include <hip/hip_runtime.h>
#include <hip/hip_bf16.h>
#include <stdint.h>
#include <type_traits>

typedef unsigned short u16;
typedef short bf16x8 __attribute__((ext_vector_type(8)));
typedef short s16x4 __attribute__((ext_vector_type(4)));
typedef short v4i16_t __attribute__((ext_vector_type(4)));
typedef float f32x16 __attribute__((ext_vector_type(16)));
typedef float f32x4 __attribute__((ext_vector_type(4)));
typedef unsigned u32x4 __attribute__((ext_vector_type(4)));
typedef unsigned u32x2 __attribute__((ext_vector_type(2)));
#define LAS __attribute__((address_space(3)))
#define DI __device__ __forceinline__
#define MFMA(a, b, c) __builtin_amdgcn_mfma_f32_32x32x16_bf16(a, b, c, 0, 0, 0)

constexpr int SEQ = 2048, DM = 512, NH = 32, HD = 128, FF = 1792;
constexpr int KST = 368;
constexpr float QSCALE = 0.08838834764831845f * 1.4426950408889634f;
#define THRV 24.0f
constexpr float THR = THRV;

constexpr size_t WS_XN = 0;
constexpr size_t WS_K = 2u << 20;
constexpr size_t WS_V = WS_K + (16u << 20);
constexpr size_t WS_X = WS_V + (16u << 20);
constexpr size_t WS_P = WS_X + (size_t)64 * KST * 1024;
constexpr size_t WS_C = WS_P + ((size_t)32 << 20);
constexpr size_t WS_END = WS_C + 48 * 256;

#define STAMP(k, i) do {} while (0)
template <int I, int N, class F> DI void sfor(F&& f) {
    if constexpr (I < N) { f(std::integral_constant<int, I>{}); sfor<I + 1, N>(f); }
}
typedef float f32x2 __attribute__((ext_vector_type(2)));
typedef __bf16 bf16x2_t __attribute__((ext_vector_type(2)));
DI unsigned cvtpk(float lo, float hi) { f32x2 v = {lo, hi}; return __builtin_bit_cast(unsigned, __builtin_convertvector(v, bf16x2_t)); }
DI float bf_lo(unsigned u) { return __uint_as_float(u << 16); }
DI float bf_hi(unsigned u) { return __uint_as_float(u & 0xffff0000u); }
DI float rbf(float f) { return bf_lo(cvtpk(f, f)); }
DI void glds16(unsigned voff, const void* sbase, unsigned lds_dst) {
    unsigned sv;
    asm volatile("s_mov_b32 %0, m0\n\ts_mov_b32 m0, %3\n\ts_nop 2\n\tglobal_load_lds_dwordx4 %1, %2\n\ts_mov_b32 m0, %0"
                 : "=&s"(sv) : "v"(voff), "s"(sbase), "s"(lds_dst) : "memory");
}
template <int OFF> DI void glds16x2(unsigned voff, const void* sbase, unsigned lds_dst) {
    unsigned sv;
    asm volatile("s_mov_b32 %0, m0\n\ts_mov_b32 m0, %3\n\ts_nop 2\n\tglobal_load_lds_dwordx4 %1, %2 offset:%4\n\tglobal_load_lds_dwordx4 %1, %2 offset:%5\n\ts_mov_b32 m0, %0"
                 : "=&s"(sv) : "v"(voff), "s"(sbase), "s"(lds_dst), "i"(OFF), "i"(OFF + 1024) : "memory");
}
template <int OFF> DI void glds16o(unsigned voff, const void* sbase, unsigned lds_dst) {
    unsigned sv;
    asm volatile("s_mov_b32 %0, m0\n\ts_mov_b32 m0, %3\n\ts_nop 2\n\tglobal_load_lds_dwordx4 %1, %2 offset:%4\n\ts_mov_b32 m0, %0"
                 : "=&s"(sv) : "v"(voff), "s"(sbase), "s"(lds_dst), "i"(OFF) : "memory");
}
template <int OFF> DI void glds16x4(unsigned voff, const void* sbase, unsigned lds_dst) {
    unsigned sv;
    asm volatile("s_mov_b32 %0, m0\n\ts_mov_b32 m0, %3\n\ts_nop 2\n\tglobal_load_lds_dwordx4 %1, %2 offset:%4\n\tglobal_load_lds_dwordx4 %1, %2 offset:%5\n\t"
                 "global_load_lds_dwordx4 %1, %2 offset:%6\n\tglobal_load_lds_dwordx4 %1, %2 offset:%7\n\ts_mov_b32 m0, %0"
                 : "=&s"(sv) : "v"(voff), "s"(sbase), "s"(lds_dst), "i"(OFF), "i"(OFF + 1024), "i"(OFF + 2048), "i"(OFF + 3072) : "memory");
}
template <int OFF> DI void gldsl(unsigned voff, const void* sbase, unsigned lds_dst) {
    asm volatile("s_mov_b32 m0, %2\n\ts_nop 0\n\tglobal_load_lds_dwordx4 %0, %1 offset:%3" :: "v"(voff), "s"(sbase), "s"(lds_dst), "i"(OFF) : "memory");
}
DI void glds4(unsigned voff, const void* sbase, unsigned lds_dst) {
    unsigned sv;
    asm volatile("s_mov_b32 %0, m0\n\ts_mov_b32 m0, %3\n\ts_nop 2\n\tglobal_load_lds_dword %1, %2\n\ts_mov_b32 m0, %0"
                 : "=&s"(sv) : "v"(voff), "s"(sbase), "s"(lds_dst) : "memory");
}
template <int OFF> DI void gld16(bf16x8& dst, unsigned voff, const void* sbase) {
    asm volatile("s_nop 4\n\tglobal_load_dwordx4 %0, %1, %2 offset:%3" : "=v"(dst) : "v"(voff), "s"(sbase), "i"(OFF) : "memory");
}
template <int OFF, class T> DI void gld16s(T& dst, unsigned voff, const void* sbase) {
    asm volatile("s_nop 4\n\tglobal_load_dwordx4 %0, %1, %2 offset:%3 sc1" : "=v"(dst) : "v"(voff), "s"(sbase), "i"(OFF) : "memory");
}
template <int OFF> DI void gld8(u32x2& dst, unsigned voff, const void* sbase) {
    asm volatile("s_nop 4\n\tglobal_load_dwordx2 %0, %1, %2 offset:%3" : "=v"(dst) : "v"(voff), "s"(sbase), "i"(OFF) : "memory");
}
template <int OFF, bool SC1 = false, class T> DI void gst16(unsigned voff, T v, void* sbase) {
    if constexpr (SC1) asm volatile("s_nop 4\n\tglobal_store_dwordx4 %0, %1, %2 offset:%3 sc1\n\ts_nop 1" :: "v"(voff), "v"(v), "s"(sbase), "i"(OFF) : "memory");
    else asm volatile("s_nop 4\n\tglobal_store_dwordx4 %0, %1, %2 offset:%3\n\ts_nop 1" :: "v"(voff), "v"(v), "s"(sbase), "i"(OFF) : "memory");
}
template <bool SC1 = false, class T> DI void gst_frag4(unsigned voff, void* sbase, T f0, T f1, T f2, T f3) {
    gst16<0, SC1>(voff, f0, sbase); gst16<1024, SC1>(voff, f1, sbase); gst16<2048, SC1>(voff, f2, sbase); gst16<3072, SC1>(voff, f3, sbase);
}
DI void arrive(unsigned* cnt) { __hip_atomic_fetch_add(cnt, 1u, __ATOMIC_RELAXED, __HIP_MEMORY_SCOPE_AGENT); }
DI void wait_count(unsigned* cnt, unsigned target) {
    for (unsigned spins = 0; spins < (1u << 20); ++spins) {
        if ((unsigned)__builtin_amdgcn_readfirstlane(__hip_atomic_load(cnt, __ATOMIC_RELAXED, __HIP_MEMORY_SCOPE_AGENT)) >= target) break;
        __builtin_amdgcn_s_sleep(2);
    }
    asm volatile("" ::: "memory");
}
DI void touch(unsigned& dummy, unsigned voff, const void* sbase) {
    asm volatile("s_nop 4\n\tglobal_load_dword %0, %1, %2" : "+v"(dummy) : "v"(voff), "s"(sbase) : "memory");
}
DI int lane_fresh() { int l; asm volatile("v_mbcnt_lo_u32_b32 %0, -1, 0\n\tv_mbcnt_hi_u32_b32 %0, -1, %0" : "=v"(l)); return l; }
template <int N> DI void waitv() { asm volatile("s_waitcnt vmcnt(%0)" :: "i"(N) : "memory"); }
template <int N> DI void waitv4(bf16x8& a, bf16x8& b, bf16x8& c, bf16x8& d) {
    asm volatile("s_waitcnt vmcnt(%4)" : "+v"(a), "+v"(b), "+v"(c), "+v"(d) : "i"(N) : "memory");
}
DI void bar() { asm volatile("s_waitcnt lgkmcnt(0)\n\ts_barrier" ::: "memory"); }
#define SB() __builtin_amdgcn_sched_barrier(0)
#define PIN(x) asm volatile("" : "+v"(x))
DI void rawbar() { SB(); asm volatile("s_barrier" ::: "memory"); SB(); }
#define PDAV 4
constexpr int PDA = PDAV;
constexpr int PD = 3;
DI s16x4 trrd(LAS const char* p) { return __builtin_bit_cast(s16x4, __builtin_amdgcn_ds_read_tr16_b64_v4i16((LAS v4i16_t*)p)); }
DI bf16x8 trfrag(LAS const char* p, int off_lo, int off_hi) {
    s16x4 a = trrd(p + off_lo), b = trrd(p + off_hi);
    return (bf16x8){a[0], a[1], a[2], a[3], b[0], b[1], b[2], b[3]};
}
DI bf16x8 ldsfrag(LAS const char* p) { return *(LAS const bf16x8*)p; }
template <class T> DI T* opaque(T* p) { asm volatile("" : "+v"(p)); return p; }
DI bf16x8 pack8(const f32x16& v, int b, float s) {
    u32x4 w = {cvtpk(v[b] * s, v[b + 1] * s), cvtpk(v[b + 2] * s, v[b + 3] * s), cvtpk(v[b + 4] * s, v[b + 5] * s), cvtpk(v[b + 6] * s, v[b + 7] * s)};
    return __builtin_bit_cast(bf16x8, w);
}
DI bf16x8 pack8n(const f32x16& v, int b) {
    u32x4 w = {cvtpk(v[b], v[b + 1]), cvtpk(v[b + 2], v[b + 3]), cvtpk(v[b + 4], v[b + 5]), cvtpk(v[b + 6], v[b + 7])};
    return __builtin_bit_cast(bf16x8, w);
}
DI float xhalf_max(float v) {
    auto rr = __builtin_amdgcn_permlane32_swap(__float_as_uint(v), __float_as_uint(v), false, false);
    return fmaxf(__uint_as_float(rr[0]), __uint_as_float(rr[1]));
}
DI float xhalf_sum(float v) {
    auto rr = __builtin_amdgcn_permlane32_swap(__float_as_uint(v), __float_as_uint(v), false, false);
    return __uint_as_float(rr[0]) + __uint_as_float(rr[1]);
}

template <int CTRL> DI float dpp_add(float v) {
    return v + __uint_as_float(__builtin_amdgcn_update_dpp(0u, __float_as_uint(v), CTRL, 0xf, 0xf, true));
}
DI float wave_sum(float v) {
    v = dpp_add<0xB1>(v); v = dpp_add<0x4E>(v); v = dpp_add<0x141>(v); v = dpp_add<0x140>(v);
    auto r16 = __builtin_amdgcn_permlane16_swap(__float_as_uint(v), __float_as_uint(v), false, false);
    v = __uint_as_float(r16[0]) + __uint_as_float(r16[1]);
    return xhalf_sum(v);
}

__global__ __launch_bounds__(512, 2) void k_kv(const u16* __restrict__ x, const u16* __restrict__ ckv, const u16* __restrict__ wuk,
                                               const u16* __restrict__ wuv, u16* __restrict__ xn, u16* __restrict__ Kf, u16* __restrict__ Vf,
                                               unsigned* __restrict__ cnt) {
    __shared__ __attribute__((aligned(1024))) char lds_[163840];
    const int tid = threadIdx.x, lane = tid & 63, r32 = lane & 31, hi = lane >> 5;
    const int w = __builtin_amdgcn_readfirstlane(tid >> 6);
    const int b = blockIdx.x, xcd = b & 7, idx = b >> 3, h = xcd * 4 + (idx & 3), j = idx >> 2;
    const unsigned lds0 = (unsigned)(uintptr_t)lds_;
    LAS const char* lds = (LAS const char*)lds_;
    STAMP(0, 0);
    bf16x8 xrow;
    gld16<0>(xrow, (unsigned)(lane * 16), (const char*)x + (size_t)(b * 8 + w) * 1024);
    const unsigned wvoff = (unsigned)((8 * w + ((lane >> 2) & 7)) * 8192 + (lane >> 5) * 64 + (lane & 3) * 16);
    const unsigned avoff0 = (unsigned)((lane >> 3) * 1024 + (((lane & 7) ^ (lane >> 4)) * 16));
    const unsigned avoff1 = (unsigned)((lane >> 3) * 1024 + (((lane & 7) ^ (4 + (lane >> 4))) * 16));
    const char* abase = (const char*)ckv + (size_t)(j * 256 + w * 32) * 1024;
    const char* wkb = (const char*)wuk + h * 256;
    const char* wvb = (const char*)wuv + h * 256;
    const unsigned ldst = lds0 + w * 2048, adst = lds0 + 65536 + w * 12288;
    bf16x8 a[32];
    auto issue_p = [&](auto ic, auto pc) __attribute__((always_inline)) {
        constexpr int st = ic, p = pc;
        if constexpr (st < 16) {
            if constexpr (p < 2) glds16(wvoff, (st < 8 ? wkb : wvb) + (size_t)(st & 7) * 64 * 8192 + p * 128, ldst + (st & 3) * 16384 + p * 1024);
            else if constexpr (st < 8) glds16((p & 1) ? avoff1 : avoff0, abase + st * 128 + (p - 2) * 8192, adst + (st % 3) * 4096 + (p - 2) * 1024);
        }
    };
    sfor<0, 3>([&](auto ic) __attribute__((always_inline)) { sfor<0, 6>([&](auto pc) __attribute__((always_inline)) { issue_p(ic, pc); }); });
    const int g16 = lane >> 4, q4 = (lane >> 2) & 3, p4 = lane & 3;
    LAS const char* rb = lds + hi * 2048 + q4 * 64 + (g16 & 1) * 32 + p4 * 8;
    LAS const char* ra[4];
#pragma unroll
    for (int u = 0; u < 4; ++u) ra[u] = opaque(lds + 65536 + w * 12288 + r32 * 128 + (((2 * u + hi) ^ ((r32 >> 1) & 7)) * 16));
    f32x16 acc[4];
    const int rg = 8 * j + w;
    bf16x8 wc[PD];
    auto rdf = [&](int st, int i) __attribute__((always_inline)) {
        const int off = (st & 3) * 16384 + (i >> 2) * 4096 + (i & 3) * 512;
        return trfrag(rb, off, off + 256);
    };
    {
        asm volatile("s_waitcnt vmcnt(18)" : "+v"(xrow) :: "memory");
        u32x4 xu = __builtin_bit_cast(u32x4, xrow);
        float v[8];
#pragma unroll
        for (int e = 0; e < 4; ++e) { v[2 * e] = bf_lo(xu[e]); v[2 * e + 1] = bf_hi(xu[e]); }
        float s = 0.f;
#pragma unroll
        for (int e = 0; e < 8; ++e) s += v[e];
        s = wave_sum(s);
        const float mean = s * (1.0f / DM);
        float q = 0.f;
#pragma unroll
        for (int e = 0; e < 8; ++e) { const float d = v[e] - mean; q += d * d; }
        q = wave_sum(q);
        const float mb = rbf(mean), vb = rbf(q * (1.0f / DM));
        const float sd = rbf(__builtin_sqrtf(rbf(vb + 1e-5f))), rs = 1.0f / sd;
        u32x4 o;
#pragma unroll
        for (int e = 0; e < 4; ++e) o[e] = cvtpk(rbf(v[2 * e] - mb) * rs, rbf(v[2 * e + 1] - mb) * rs);
        const int row = b * 8 + w;
        *(u32x4*)((char*)xn + (size_t)(row >> 5) * 32768 + lane * 512 + (row & 31) * 16) = o;
    }
    if (b == 0 && tid < 48) cnt[tid * 64] = 0u;
    waitv<12>(); rawbar();
    STAMP(0, 1);
#pragma unroll
    for (int u = 0; u < 4; ++u) a[u] = ldsfrag(ra[u]);
#pragma unroll
    for (int i = 0; i < PD; ++i) wc[i] = rdf(0, i);
    sfor<0, 16>([&](auto ic) __attribute__((always_inline)) {
        constexpr int st = ic;
        constexpr int cnt = st <= 5 ? 6 : st <= 7 ? 2 : st <= 9 ? 10 : st <= 13 ? 2 : 0;
        if constexpr (st < 15) { waitv<cnt>(); rawbar(); }
        bf16x8 wf[16 + PD];
#pragma unroll
        for (int i = 0; i < PD; ++i) wf[i] = wc[i];
        sfor<0, 16>([&](auto iic) __attribute__((always_inline)) {
            constexpr int i = iic;
            if constexpr (i + PD < 16) wf[i + PD] = rdf(st, i + PD);
            else if constexpr (st < 15) wf[i + PD] = rdf(st + 1, i + PD - 16);
            if constexpr (st < 7 && i >= 8 && i < 12) a[4 * (st + 1) + i - 8] = ldsfrag(ra[i - 8] + ((st + 1) % 3) * 4096);
            const f32x16 cin = ((st & 7) == 0 && i < 4) ? f32x16{} : acc[i & 3];
            if constexpr (st < 8) acc[i & 3] = MFMA(wf[i], a[4 * (st & 7) + (i >> 2)], cin);
            else acc[i & 3] = MFMA(a[4 * (st & 7) + (i >> 2)], wf[i], cin);
            if constexpr (i >= 1 && i < 7) issue_p(std::integral_constant<int, st + 3>{}, std::integral_constant<int, i - 1>{});
            SB();
        });
#pragma unroll
        for (int i = 0; i < PD; ++i) wc[i] = wf[16 + i];
        if constexpr (st == 7) STAMP(0, 2);
        if constexpr (st == 15) STAMP(0, 3);
        if constexpr (st == 7) {
            char* dst = (char*)Kf + (size_t)(h * 64 + rg) * 8192;
            gst_frag4((unsigned)(lane * 16), dst, pack8n(acc[0], 0), pack8n(acc[0], 8), pack8n(acc[1], 0), pack8n(acc[1], 8));
            gst_frag4((unsigned)(lane * 16), dst + 4096, pack8n(acc[2], 0), pack8n(acc[2], 8), pack8n(acc[3], 0), pack8n(acc[3], 8));
        }
        if constexpr (st == 15) {
            char* dst = (char*)Vf + (size_t)(h * 64 + rg) * 8192;
            gst_frag4((unsigned)(lane * 16), dst, pack8n(acc[0], 0), pack8n(acc[1], 0), pack8n(acc[2], 0), pack8n(acc[3], 0));
            gst_frag4((unsigned)(lane * 16), dst + 4096, pack8n(acc[0], 8), pack8n(acc[1], 8), pack8n(acc[2], 8), pack8n(acc[3], 8));
        }
    });
    STAMP(0, 4);
}

constexpr int A_K = 0, A_V = 65536, A_Q = 131072, A_LDS = 163840;

DI float max3f(float a, float b, float c) { return __builtin_fmaxf(__builtin_fmaxf(a, b), c); }
DI void cmask(f32x16& p0, f32x16& p1, int dq) {
    const float NEG = -__builtin_inff();
#pragma unroll
    for (int r = 0; r < 16; ++r) {
        const int c = (r & 3) + 8 * (r >> 2);
        if (c > dq) p0[r] = NEG;
        if (c + 32 > dq) p1[r] = NEG;
    }
}

__global__ __launch_bounds__(512, 2) void k_main(const u16* __restrict__ xn, const u16* __restrict__ wq, const u16* __restrict__ w1,
                                                 const u16* __restrict__ b1, const u16* __restrict__ Kf, const u16* __restrict__ Vf,
                                                 u16* Xf, const u16* __restrict__ wo, const u16* __restrict__ w2, float* P,
                                                 unsigned* cnt, const u16* __restrict__ x, const u16* __restrict__ b2, u16* __restrict__ out) {
    __shared__ __attribute__((aligned(1024))) char lds_[A_LDS];
    const int tid = threadIdx.x, lane = tid & 63, r32 = lane & 31, hi = lane >> 5;
    const int w = __builtin_amdgcn_readfirstlane(tid >> 6);
    const int b = blockIdx.x, xcd = b & 7, idx = b >> 3, h = xcd * 4 + (idx & 3), pi = idx >> 2;
    const int grp = w >> 2, wr = w & 3;
    const int qbig = 15 - pi, qsm = pi;
    const unsigned lds0 = (unsigned)(uintptr_t)lds_;
    LAS char* lds = (LAS char*)lds_;
    const int n1 = 2 * pi + 2;
    const int hl = idx & 3, hc0 = 224 * xcd + (hl < 3 ? 64 * hl : 160);
    STAMP(1, 0);
    const int rg_own = (grp == 0 ? qbig : qsm) * 4 + wr;
    const unsigned wvoff = (unsigned)((8 * w + ((lane >> 2) & 7)) * 8192 + (lane >> 5) * 64 + (lane & 3) * 16);
    const unsigned hvoff = (unsigned)((16 * w + ((lane >> 2) & 7)) * (FF * 2) + (lane >> 5) * 64 + (lane & 3) * 16);
    const unsigned tvoff = (unsigned)(lane * 16);
    const char* abase = (const char*)xn + (size_t)rg_own * 32768;
    const char* wqb = (const char*)wq + h * 256;
    const char* w1b = (const char*)w1 + hc0 * 2;
    const char* kgb = (const char*)Kf + (size_t)h * 32 * 16384;
    const char* vgb = (const char*)Vf + (size_t)h * 32 * 16384;
    const unsigned ldst = lds0 + w * 2048;
    auto issue_step = [&](const char* gb, unsigned region, int s, auto pc) __attribute__((always_inline)) {
        constexpr int p = pc;
        if (s <= 16 && (p == 0 || s >= n1)) {
            const int off = s < n1 ? s * 16384 + w * 2048 : (2 * s - n1) * 16384 + w * 4096;
            glds16x2<2048 * p>(tvoff, gb + off, lds0 + region + (unsigned)(off & 65535));
        }
    };
    constexpr std::integral_constant<int, 0> P0{}; constexpr std::integral_constant<int, 1> P1{};
    bf16x8 qf[8];
    {
        bf16x8 a[32];
        auto issue_p = [&](auto ic, auto pc) __attribute__((always_inline)) {
            constexpr int st = ic, p = pc;
            if constexpr (st < 8) {
                if constexpr (p < 2) glds16(wvoff, wqb + (size_t)st * 64 * 8192 + p * 128, ldst + A_V + (st & 3) * 16384 + p * 1024);
                else gld16<(p - 2) * 1024>(a[4 * st + p - 2], tvoff, abase + st * 4096);
            } else if constexpr (st < 12) {
                if constexpr (p < 2) glds16(hvoff, w1b + (size_t)((st - 8) * 128 + 8 * p) * (FF * 2), ldst + A_V + (st & 3) * 16384 + p * 1024);
                if constexpr (st == 11 && p == 2) glds4((unsigned)(r32 * 4), (const char*)b1 + hc0 * 2, lds0 + A_K + 3 * 16384 + w * 256);
            } else if constexpr (st < 14) {
                if constexpr (p == 0) issue_step(kgb, A_K, st - 12, P0);
            }
        };
        sfor<0, 3>([&](auto ic) __attribute__((always_inline)) { sfor<0, 6>([&](auto pc) __attribute__((always_inline)) { issue_p(ic, pc); }); });
        const int g16 = lane >> 4, q4 = (lane >> 2) & 3, p4 = lane & 3;
        LAS const char* rbq = opaque(lds + A_V + hi * 2048 + q4 * 64 + (g16 & 1) * 32 + p4 * 8);
        LAS const char* rbh = opaque(lds + A_V + hi * 1024 + q4 * 64 + (g16 & 1) * 32 + p4 * 8);
        auto rdf = [&](int st, int i) __attribute__((always_inline)) {
            if (st < 8) { const int off = (st & 3) * 16384 + (i >> 2) * 4096 + (i & 3) * 512; return trfrag(rbq, off, off + 256); }
            else { const int off = (st & 3) * 16384 + (i >> 1) * 2048 + (i & 1) * 512; return trfrag(rbh, off, off + 256); }
        };
        f32x16 acc[4];
        bf16x8 wc[PD];
        waitv<12>(); rawbar();
        STAMP(1, 1);
#pragma unroll
        for (int i = 0; i < PD; ++i) wc[i] = rdf(0, i);
        sfor<0, 12>([&](auto ic) __attribute__((always_inline)) {
            constexpr int st = ic;
            constexpr int cnt = st <= 5 ? 6 : st <= 8 ? 2 : st == 9 ? 3 : st == 10 ? 2 : 4;
            if constexpr (st < 8) waitv4<cnt>(a[4 * st], a[4 * st + 1], a[4 * st + 2], a[4 * st + 3]); else waitv<cnt>();
            rawbar();
            bf16x8 wf[16 + PD];
#pragma unroll
            for (int i = 0; i < PD; ++i) wf[i] = wc[i];
            sfor<0, 16>([&](auto iic) __attribute__((always_inline)) {
                constexpr int i = iic;
                if constexpr (i + PD < 16) wf[i + PD] = rdf(st, i + PD);
                else if constexpr (st < 11) wf[i + PD] = rdf(st + 1, i + PD - 16);
                if constexpr (st < 8) {
                    const f32x16 cin = (st == 0 && i < 4) ? f32x16{} : acc[i & 3];
                    acc[i & 3] = MFMA(wf[i], a[4 * st + (i >> 2)], cin);
                } else {
                    const f32x16 cin = (st == 8 && i < 2) ? f32x16{} : acc[i & 1];
                    acc[i & 1] = MFMA(wf[i], a[8 * (st - 8) + (i >> 1)], cin);
                }
                if constexpr (i >= 1 && i < 7) issue_p(std::integral_constant<int, st + 3>{}, std::integral_constant<int, i - 1>{});
                SB();
            });
#pragma unroll
            for (int i = 0; i < PD; ++i) wc[i] = wf[16 + i];
            if constexpr (st == 7) {
#pragma unroll
                for (int nt = 0; nt < 4; ++nt) { qf[2 * nt] = pack8(acc[nt], 0, QSCALE); qf[2 * nt + 1] = pack8(acc[nt], 8, QSCALE); }
            }
        });
#pragma unroll
        for (int nt = 0; nt < 2; ++nt)
#pragma unroll
            for (int g = 0; g < 4; ++g) {
                const u32x2 bb = *(LAS const u32x2*)(lds + A_K + 3 * 16384 + w * 256 + hi * 8 + (nt * 4 + g) * 16);
                const float bv[4] = {bf_lo(bb[0]), bf_hi(bb[0]), bf_lo(bb[1]), bf_hi(bb[1])};
#pragma unroll
                for (int e = 0; e < 4; ++e) { const float t = fmaxf(acc[nt][4 * g + e] + bv[e], 0.f); acc[nt][4 * g + e] = t * t; }
            }
        char* hdst = (char*)Xf + ((size_t)rg_own * KST + 256 + (hc0 >> 4)) * 1024;
        if (hl < 3) { gst16<0, true>(tvoff, pack8n(acc[0], 0), hdst); gst16<1024, true>(tvoff, pack8n(acc[0], 8), hdst); }
        gst16<2048, true>(tvoff, pack8n(acc[1], 0), hdst); gst16<3072, true>(tvoff, pack8n(acc[1], 8), hdst);
    }
    LAS const char* kb0 = lds + A_K + lane * 16;
    LAS const char* vb0 = opaque(lds + A_V + lane * 16);
    LAS char* qx = opaque(lds + A_Q + wr * 8192 + lane * 16);
    if (grp == 0) {
#pragma unroll
        for (int ks = 0; ks < 8; ++ks) *(LAS bf16x8*)(qx + ks * 1024) = qf[ks];
    }
    const int rg_big = qbig * 4 + wr;
    f32x16 o[4] = {f32x16{}, f32x16{}, f32x16{}, f32x16{}};
    f32x16 negm = f32x16{};
    float mh = 0.f, l = 0.f, il_sm = 0.f;
    int row0 = rg_own * 32;
    f32x16 sA0, sA1, sB0, sB1;
    u32x4 pw[4];
    bf16x8 kc[PDA], vc[PDA];
    bool pend = false; float fpend = 1.f;
    auto tile_of = [&](int s) __attribute__((always_inline)) { return s < n1 ? s : 2 * s - n1 + grp; };
    auto decide = [&](f32x16& c0, f32x16& c1, float rm, bool first) __attribute__((always_inline)) {
        rm = xhalf_max(rm);
        if (__builtin_expect(__any(rm > THR) || first, 0)) {
            const float dl = first ? __builtin_fmaxf(rm, -1e30f) : __builtin_fmaxf(rm, 0.f);
            mh += dl;
#pragma unroll
            for (int r = 0; r < 16; ++r) { c0[r] -= dl; c1[r] -= dl; negm[r] = -mh; }
            fpend = first ? 0.f : __builtin_amdgcn_exp2f(-dl);
            l *= fpend; pend = true;
        }
    };
    STAMP(1, 2);
    waitv<2>(); bar();
    issue_step(vgb, A_V, 0, P0);
    {
#pragma unroll
        for (int ks = 0; ks < 8; ++ks) {
            const bf16x8 k0 = ldsfrag(kb0 + ks * 1024), k1 = ldsfrag(kb0 + 8192 + ks * 1024);
            sA0 = MFMA(k0, qf[ks], ks == 0 ? f32x16{} : sA0);
            sA1 = MFMA(k1, qf[ks], ks == 0 ? f32x16{} : sA1);
        }
        waitv<2>(); bar();
        issue_step(kgb, A_K, 2, P0); issue_step(kgb, A_K, 2, P1);
        if (63 > row0) cmask(sA0, sA1, row0 + r32 - 4 * hi);
        float rm = max3f(sA0[0], sA0[1], sA1[0]);
        rm = __builtin_fmaxf(rm, sA1[1]);
#pragma unroll
        for (int r = 2; r < 16; ++r) rm = max3f(rm, sA0[r], sA1[r]);
        decide(sA0, sA1, rm, true);
        pend = false;
#pragma unroll
        for (int r = 0; r < 16; ++r) { sA0[r] = __builtin_amdgcn_exp2f(sA0[r]); sA1[r] = __builtin_amdgcn_exp2f(sA1[r]); }
#pragma unroll
        for (int i = 0; i < PDA; ++i) kc[i] = ldsfrag(kb0 + 16384 + (i & 1) * 8192 + (i >> 1) * 1024);
        waitv<0>();
        bar();
        if (tid == 0) arrive(cnt + xcd * 64);
    }
#define PH(k) do {} while (0)
    int tV, tK, tKd, tSl;
    {
        const int lf = lane_fresh(), s_ = lf & 31, up = lf >> 5;
        auto tbase = [&](int s) __attribute__((always_inline)) { const int sc = s < 16 ? s : 16, d = sc - n1; return (sc + (d > 0 ? d : 0)) << 14; };
        const int tv = tbase(s_), tk = tbase(s_ + 2), vtwo = s_ >= n1 ? 1 : 0, ktwo = s_ + 2 >= n1 ? 1 : 0;
        tV = tv + (w << (11 + vtwo)) + 2048 * (vtwo & up);
        tK = tk + (w << (11 + ktwo)) + 2048 * (ktwo & up);
        tKd = (int)(lds0 + A_K) + (((s_ + 2 <= 16 ? tk : tv) & 65535) + (w << (11 + ktwo))) + 2048 * (ktwo & up);
        tSl = (((tv >> 14) + (vtwo & grp)) & 3) << 14;
    }
    const unsigned spec = (1u << 16) | (grp ? (7u << (n1 - 2)) : 0u);
    const int nsw = grp ? n1 : -1;
    LAS const char* kb = kb0 + 16384;
    LAS const char* vb = vb0;
    LAS const char* kbn = kb0 + (tile_of(2) & 3) * 16384;
    auto step = [&](auto evn, f32x16& c0, f32x16& c1, f32x16& p0, f32x16& p1, const int s) __attribute__((always_inline)) {
        waitv<4>();
        rawbar();
        PH(0);
        {
            bf16x8 kf[16];
#pragma unroll
            for (int i = 0; i < PDA; ++i) kf[i] = kc[i];
            float sa = 0.f, sb = 0.f;
            unsigned dvo = 0, dm0 = 0;
            sfor<0, 16>([&](auto iic) __attribute__((always_inline)) {
                constexpr int i = iic;
                if constexpr (i + PDA < 16) kf[i + PDA] = ldsfrag(kb + ((i + PDA) & 1) * 8192 + ((i + PDA) >> 1) * 1024);
                else vc[i + PDA - 16] = ldsfrag(vb + (i + PDA - 16) * 1024);
                SB();
                if constexpr (i & 1) c1 = MFMA(kf[i], qf[i >> 1], i < 2 ? negm : c1);
                else c0 = MFMA(kf[i], qf[i >> 1], i < 2 ? negm : c0);
                const float e0 = i < 8 ? p0[2 * i] : p1[2 * i - 16], e1 = i < 8 ? p0[2 * i + 1] : p1[2 * i - 15];
                sa += e0; sb += e1;
                pw[i >> 2][i & 3] = cvtpk(e0, e1);
                PIN(sa); PIN(sb); PIN(pw[i >> 2]);
                if constexpr ((i & 7) == 1) { const int off = __builtin_amdgcn_readlane(tV, i < 8 ? s : s + 32);
                    dvo = tvoff + off; dm0 = lds0 + A_V + (off & 65535); PIN(dvo); }
                if constexpr ((i & 3) == 2) gldsl<1024 * ((i >> 2) & 1)>(dvo, vgb, dm0);
                SB();
            });
            l += sa + sb;
        }
        PH(1);
        waitv<4>();
        rawbar();
        PH(2);
        const bool sw = (bool)evn && s == nsw;
        const bool swn = !(bool)evn && s + 1 == nsw;
        {
            bf16x8 vf[16];
#pragma unroll
            for (int i = 0; i < PDA; ++i) vf[i] = vc[i];
            float ra = 0.f, rb2 = 0.f;
            unsigned dvo = 0, dm0 = 0;
            sfor<0, 16>([&](auto iic) __attribute__((always_inline)) {
                constexpr int i = iic;
                if constexpr (i + PDA < 16) vf[i + PDA] = ldsfrag(vb + (i + PDA) * 1024);
                else kc[i + PDA - 16] = ldsfrag(kbn + ((i + PDA - 16) & 1) * 8192 + ((i + PDA - 16) >> 1) * 1024);
                SB();
                o[i & 3] = MFMA(vf[i], __builtin_bit_cast(bf16x8, pw[i >> 2]), o[i & 3]);
                if constexpr (i == 0) {
                    if (__builtin_expect((spec >> s) & 1, 0)) {
                        if (sw) { il_sm = 1.0f / xhalf_sum(l); l = 0.f; }
                        const int lf = lane_fresh(); cmask(c0, c1, row0 + (lf & 31) - 64 * tile_of(s) - 4 * (lf >> 5));
                    }
                } else if constexpr (i < 5) {
                    constexpr int q = i - 1;
                    if constexpr (q == 0) { ra = max3f(c0[0], c0[1], c1[0]); rb2 = max3f(c0[2], c0[3], c1[1]); ra = max3f(ra, c1[2], c1[3]); }
                    else { ra = max3f(ra, c0[4 * q], c0[4 * q + 1]); rb2 = max3f(rb2, c0[4 * q + 2], c0[4 * q + 3]);
                           ra = max3f(ra, c1[4 * q], c1[4 * q + 1]); rb2 = max3f(rb2, c1[4 * q + 2], c1[4 * q + 3]); }
                    PIN(ra); PIN(rb2);
                } else if constexpr (i == 5) {
                    decide(c0, c1, __builtin_fmaxf(ra, rb2), sw);
                } else {
                    constexpr int e_lo = (32 * (i - 6)) / 10, e_hi = (32 * (i - 5)) / 10;
#pragma unroll
                    for (int e = e_lo; e < e_hi; ++e) {
                        if (e < 16) c0[e] = __builtin_amdgcn_exp2f(c0[e]); else c1[e - 16] = __builtin_amdgcn_exp2f(c1[e - 16]);
                    }
                    PIN(c0); PIN(c1);
                }
                if constexpr ((i & 7) == 1) { const int off = __builtin_amdgcn_readlane(tK, i < 8 ? s : s + 32);
                    dvo = tvoff + off; dm0 = (unsigned)__builtin_amdgcn_readlane(tKd, i < 8 ? s : s + 32); PIN(dvo); }
                if constexpr ((i & 3) == 2) gldsl<1024 * ((i >> 2) & 1)>(dvo, kgb, dm0);
                if constexpr (i == 15 - PDA) { vb = vb0 + __builtin_amdgcn_readlane(tSl, s); PIN(vb); }
                if constexpr (i == 15) { kb = kbn; kbn = kb0 + __builtin_amdgcn_readlane(tSl, s + 2); PIN(kb); PIN(kbn); }
                SB();
            });
        }
        PH(3);
        if (__builtin_expect(pend || swn, 0)) {
            if (sw) {
                char* dst = (char*)Xf + ((size_t)rg_own * KST + 8 * h) * 1024;
                gst_frag4<true>(tvoff, dst, pack8(o[0], 0, il_sm), pack8(o[0], 8, il_sm), pack8(o[1], 0, il_sm), pack8(o[1], 8, il_sm));
                gst_frag4<true>(tvoff, dst + 4096, pack8(o[2], 0, il_sm), pack8(o[2], 8, il_sm), pack8(o[3], 0, il_sm), pack8(o[3], 8, il_sm));
            }
            if (pend) {
#pragma unroll
                for (int d = 0; d < 4; ++d)
#pragma unroll
                    for (int r = 0; r < 16; ++r) o[d][r] *= fpend;
                pend = false;
            }
            if (swn) {
#pragma unroll
                for (int ks = 0; ks < 8; ++ks) qf[ks] = *(LAS const bf16x8*)(qx + ks * 1024);
                negm = f32x16{}; mh = 0.f; row0 = rg_big * 32;
            }
        }
        PH(4);
    };
    STAMP(1, 3);
    unsigned dummy = 0;
    for (int s = 1; s < 17; s += 2) {
        if (s == 9) {
            const int lf = lane_fresh(), r = pi * 92 + w * 12 + (lf >> 1), rr = (lf >> 1) < 12 && r < 736 ? r : pi * 92;
            const char* wb = rr < 224 ? (const char*)w2 + (size_t)xcd * 224 * 1024 : (const char*)wo + (size_t)xcd * 512 * 1024 - 224 * 1024;
            asm volatile("global_load_dword %0, %1, off" : "+v"(dummy) : "v"(wb + (size_t)rr * 1024 + hl * 256 + (lf & 1) * 128) : "memory");
        }
        if (s == 15 && w == 0) wait_count(cnt + xcd * 64, 32);
        step(std::false_type{}, sB0, sB1, sA0, sA1, s);
        step(std::true_type{}, sA0, sA1, sB0, sB1, s + 1);
    }
    STAMP(1, 4);
    {
        const int lane = lane_fresh(), hi = lane >> 5;
        const int xs = xcd, rb = idx >> 2, cb = idx & 3;
        const unsigned wvo = (unsigned)((8 * w + ((lane >> 2) & 7)) * 1024 + (lane >> 5) * 64 + (lane & 3) * 16);
        const unsigned wvo_h = (unsigned)((8 * (w & 3) + ((lane >> 2) & 7)) * 1024 + (lane >> 5) * 64 + (lane & 3) * 16);
        const int rg = rb * 8 + w;
        const unsigned avo = (unsigned)(lane * 16);
        const char* xa = (const char*)Xf + (size_t)rg * (KST * 1024) + (size_t)xs * 32 * 1024;
        const char* xh = (const char*)Xf + (size_t)rg * (KST * 1024) + (size_t)(256 + 14 * xs) * 1024;
        const char* wob = (const char*)wo + (size_t)xs * 512 * 1024 + cb * 256;
        const char* w2b = (const char*)w2 + (size_t)xs * 224 * 1024 + cb * 256;
        bf16x8 a[3][4];
        auto issueW = [&](auto ic) __attribute__((always_inline)) {
            constexpr int st = ic;
            const char* src = st < 4 ? w2b + (size_t)st * 65536 : wob + (size_t)(st - 4) * 65536;
            const unsigned vo = st == 3 ? wvo_h : wvo;
            glds16(vo, src, ldst + A_K + (st & 3) * 16384);
            glds16(vo, src + 128, ldst + A_K + (st & 3) * 16384 + 1024);
        };
        auto issueA = [&](auto ic) __attribute__((always_inline)) {
            constexpr int st = ic;
            const char* src = st < 4 ? xh + st * 4096 : xa + (st - 4) * 4096;
            gld16s<0>(a[st % 3][0], avo, src);
            gld16s<1024>(a[st % 3][1], avo, src);
            if constexpr (st != 3) { gld16s<2048>(a[st % 3][2], avo, src); gld16s<3072>(a[st % 3][3], avo, src); }
        };
        asm volatile("s_waitcnt vmcnt(0)" : "+v"(dummy) :: "memory"); bar();
        issueW(std::integral_constant<int, 0>{}); issueA(std::integral_constant<int, 0>{});
        issueW(std::integral_constant<int, 1>{}); issueA(std::integral_constant<int, 1>{});
        issueW(std::integral_constant<int, 2>{});
        {
            float sa = 0.f;
#pragma unroll
            for (int r = 0; r < 16; ++r) sa += sA0[r] + sA1[r];
            l += sa;
            const bf16x8 pf[4] = {pack8n(sA0, 0), pack8n(sA0, 8), pack8n(sA1, 0), pack8n(sA1, 8)};
#pragma unroll
            for (int i = 0; i < 16; ++i) o[i & 3] = MFMA(ldsfrag(vb + i * 1024), pf[i >> 2], o[i & 3]);
        }
        bar();
        l = xhalf_sum(l);
        {
            LAS char* mine = opaque(lds + A_V + w * 8192 + lane * 16);
            LAS char* oth = opaque(lds + A_V + (w ^ 4) * 8192 + lane * 16);
            LAS float* ml = opaque((LAS float*)(lds + A_Q) + lane);
#pragma unroll
            for (int dd = 0; dd < 2; ++dd)
#pragma unroll
                for (int g = 0; g < 4; ++g) {
                    const f32x16& sv = grp == 0 ? o[2 + dd] : o[dd];
                    *(LAS f32x4*)(mine + (dd * 4 + g) * 1024) = (f32x4){sv[4 * g], sv[4 * g + 1], sv[4 * g + 2], sv[4 * g + 3]};
                }
            ml[w * 128] = mh; ml[w * 128 + 64] = l;
            bar();
            const float m2 = ml[(w ^ 4) * 128], l2 = ml[(w ^ 4) * 128 + 64];
            const float mn = fmaxf(mh, m2), f1 = __builtin_amdgcn_exp2f(mh - mn), f2 = __builtin_amdgcn_exp2f(m2 - mn);
            const float il = 1.0f / (l * f1 + l2 * f2), g1 = f1 * il, g2 = f2 * il;
            f32x16 sv[2];
#pragma unroll
            for (int dd = 0; dd < 2; ++dd) {
                sv[dd] = grp == 0 ? o[dd] : o[2 + dd];
#pragma unroll
                for (int g = 0; g < 4; ++g) {
                    const f32x4 t = *(LAS const f32x4*)(oth + (dd * 4 + g) * 1024);
#pragma unroll
                    for (int e = 0; e < 4; ++e) sv[dd][4 * g + e] = sv[dd][4 * g + e] * g1 + t[e] * g2;
                }
            }
            gst_frag4<true>(avo, (char*)Xf + ((size_t)rg_big * KST + 8 * h + 4 * grp) * 1024,
                            pack8n(sv[0], 0), pack8n(sv[0], 8), pack8n(sv[1], 0), pack8n(sv[1], 8));
        }
        STAMP(1, 5);
        waitv<0>(); bar();
        if (tid == 0) arrive(cnt + (8 + xcd) * 64);
        const int g16 = lane >> 4, q4 = (lane >> 2) & 3, p4 = lane & 3;
        LAS const char* rbp = opaque(lds + A_K + hi * 256 + q4 * 64 + 8 * (2 * (g16 & 1) + (p4 >> 1) + 4 * (p4 & 1)));
        f32x16 acc[4];
        bf16x8 wc[PD];
        auto rdf = [&](int st, int i) __attribute__((always_inline)) {
            const int off = (st & 3) * 16384 + (i >> 2) * 4096 + (i & 3) * 512;
            return trfrag(rbp, off, off + 2048);
        };
#pragma unroll
        for (int i = 0; i < PD; ++i) wc[i] = rdf(0, i);
        sfor<0, 12>([&](auto ic) __attribute__((always_inline)) {
            constexpr int st = ic;
            constexpr int cnt_ = st == 2 ? 4 : st <= 9 ? 6 : st == 10 ? 4 : 0;
            if constexpr (st == 2) { if (w == 0) wait_count(cnt + (8 + xcd) * 64, 32); }
            waitv4<cnt_>(a[st % 3][0], a[st % 3][1], a[st % 3][2], a[st % 3][3]);
            rawbar();
            if constexpr (st + 2 < 12) issueA(std::integral_constant<int, st + 2>{});
            if constexpr (st + 3 < 12) issueW(std::integral_constant<int, st + 3>{});
            SB();
            constexpr int NF = st == 3 ? 8 : 16;
            bf16x8 wf[16 + PD];
#pragma unroll
            for (int i = 0; i < PD; ++i) wf[i] = wc[i];
#pragma unroll
            for (int i = 0; i < NF; ++i) {
                if (i + PD < NF) wf[i + PD] = rdf(st, i + PD);
                else if (st < 11) wf[i + PD] = rdf(st + 1, i + PD - NF);
                const f32x16 cin = (st == 0 && i < 4) ? f32x16{} : acc[i & 3];
                acc[i & 3] = MFMA(wf[i], a[st % 3][i >> 2], cin);
                SB();
            }
#pragma unroll
            for (int i = 0; i < PD; ++i) wc[i] = wf[NF + i];
        });
        STAMP(2, 0);
        char* pdst = (char*)P + (((size_t)xs * 64 + rg) * 16 + cb * 4) * 2048;
        gst_frag4<true>(avo, pdst, pack8n(acc[0], 0), pack8n(acc[0], 8), pack8n(acc[1], 0), pack8n(acc[1], 8));
        gst_frag4<true>(avo, pdst + 4096, pack8n(acc[2], 0), pack8n(acc[2], 8), pack8n(acc[3], 0), pack8n(acc[3], 8));
        waitv<0>(); bar();
        if (tid == 0) arrive(cnt + (16 + idx) * 64);
        const int row = (rb * 8 + xs) * 32 + (lane & 31), col = cb * 128 + (w >> 1) * 32 + hi * 16 + 8 * (w & 1);
        u32x4 xv = *(const u32x4*)(x + (size_t)row * 512 + col), bv = *(const u32x4*)(b2 + col);
        PIN(xv); PIN(bv);
        if (w == 0) wait_count(cnt + (16 + idx) * 64, 8);
        bar();
        STAMP(2, 1);
        {
            const int rgr = rb * 8 + xs;
            const char* src = (const char*)P + ((size_t)rgr * 16 + cb * 4) * 2048 + w * 1024;
            u32x4 pv[8];
#pragma unroll
            for (int k = 0; k < 8; ++k) gld16s<0>(pv[k], avo, src + (size_t)k * (64 * 16 * 2048));
            asm volatile("s_waitcnt vmcnt(0)" : "+v"(pv[0]), "+v"(pv[1]), "+v"(pv[2]), "+v"(pv[3]), "+v"(pv[4]), "+v"(pv[5]), "+v"(pv[6]), "+v"(pv[7]) :: "memory");
            u32x4 o4;
#pragma unroll
            for (int e = 0; e < 4; ++e) {
                float lo = bf_lo(xv[e]) + bf_lo(bv[e]), hh = bf_hi(xv[e]) + bf_hi(bv[e]);
#pragma unroll
                for (int k = 0; k < 8; ++k) { lo += bf_lo(pv[k][e]); hh += bf_hi(pv[k][e]); }
                o4[e] = cvtpk(lo, hh);
            }
            *(u32x4*)(out + (size_t)row * 512 + col) = o4;
        }
        STAMP(2, 2); STAMP(2, 3);
    }
}

extern "C" void kernel_launch(void* const* d_in, const int* in_sizes, int n_in, void* d_out, int out_size, void* d_ws, size_t ws_size,
                              hipStream_t stream) {
    (void)in_sizes; (void)n_in; (void)out_size; (void)ws_size;
    const u16* x = (const u16*)d_in[0];
    const u16* ckv = (const u16*)d_in[1];
    const u16* wq = (const u16*)d_in[2];
    const u16* wuk = (const u16*)d_in[3];
    const u16* wuv = (const u16*)d_in[4];
    const u16* wo = (const u16*)d_in[5];
    const u16* w1 = (const u16*)d_in[7];
    const u16* b1 = (const u16*)d_in[8];
    const u16* w2 = (const u16*)d_in[9];
    const u16* b2 = (const u16*)d_in[10];
    char* ws = (char*)d_ws;
    u16* xn = (u16*)(ws + WS_XN);
    u16* Kf = (u16*)(ws + WS_K);
    u16* Vf = (u16*)(ws + WS_V);
    u16* Xf = (u16*)(ws + WS_X);
    float* P = (float*)(ws + WS_P);
    unsigned* cnt = (unsigned*)(ws + WS_C);
    k_kv<<<256, 512, 0, stream>>>(x, ckv, wuk, wuv, xn, Kf, Vf, cnt);
    k_main<<<256, 512, 0, stream>>>(xn, wq, w1, b1, Kf, Vf, Xf, wo, w2, P, cnt, x, b2, (u16*)d_out);
}
```

```cpp
#include <hip/hip_runtime.h>
#include <hip/hip_bf16.h>
#include <stdint.h>
#include <type_traits>

typedef unsigned short u16;
typedef short bf16x8 __attribute__((ext_vector_type(8)));
typedef short s16x4 __attribute__((ext_vector_type(4)));
typedef short v4i16_t __attribute__((ext_vector_type(4)));
typedef float f32x16 __attribute__((ext_vector_type(16)));
typedef float f32x4 __attribute__((ext_vector_type(4)));
typedef unsigned u32x4 __attribute__((ext_vector_type(4)));
typedef unsigned u32x2 __attribute__((ext_vector_type(2)));
#define LAS __attribute__((address_space(3)))
#define DI __device__ __forceinline__
#define MFMA(a, b, c) __builtin_amdgcn_mfma_f32_32x32x16_bf16(a, b, c, 0, 0, 0)

constexpr int SEQ = 2048, DM = 512, NH = 32, HD = 128, FF = 1792;
constexpr int KST = 368;
constexpr float QSCALE = 0.08838834764831845f * 1.4426950408889634f;
constexpr float THR = 24.0f;
constexpr float OVF = 100.0f;

constexpr size_t WS_XN = 0;
constexpr size_t WS_K = 2u << 20;
constexpr size_t WS_V = WS_K + (16u << 20);
constexpr size_t WS_X = WS_V + (16u << 20);
constexpr size_t WS_P = WS_X + (size_t)64 * KST * 1024;
constexpr size_t WS_C = WS_P + ((size_t)32 << 20);
constexpr size_t WS_END = WS_C + 48 * 256;

template <int I, int N, class F> DI void sfor(F&& f) {
    if constexpr (I < N) { f(std::integral_constant<int, I>{}); sfor<I + 1, N>(f); }
}
typedef float f32x2 __attribute__((ext_vector_type(2)));
typedef __bf16 bf16x2_t __attribute__((ext_vector_type(2)));
DI unsigned cvtpk(float lo, float hi) { f32x2 v = {lo, hi}; return __builtin_bit_cast(unsigned, __builtin_convertvector(v, bf16x2_t)); }
DI float bf_lo(unsigned u) { return __uint_as_float(u << 16); }
DI float bf_hi(unsigned u) { return __uint_as_float(u & 0xffff0000u); }
DI float rbf(float f) { return bf_lo(cvtpk(f, f)); }
DI void glds16(unsigned voff, const void* sbase, unsigned lds_dst) {
    unsigned sv;
    asm volatile("s_mov_b32 %0, m0\n\ts_mov_b32 m0, %3\n\ts_nop 2\n\tglobal_load_lds_dwordx4 %1, %2\n\ts_mov_b32 m0, %0"
                 : "=&s"(sv) : "v"(voff), "s"(sbase), "s"(lds_dst) : "memory");
}
template <int OFF> DI void glds16x2(unsigned voff, const void* sbase, unsigned lds_dst) {
    unsigned sv;
    asm volatile("s_mov_b32 %0, m0\n\ts_mov_b32 m0, %3\n\ts_nop 2\n\tglobal_load_lds_dwordx4 %1, %2 offset:%4\n\tglobal_load_lds_dwordx4 %1, %2 offset:%5\n\ts_mov_b32 m0, %0"
                 : "=&s"(sv) : "v"(voff), "s"(sbase), "s"(lds_dst), "i"(OFF), "i"(OFF + 1024) : "memory");
}
template <int OFF> DI void gldsl(unsigned voff, const void* sbase, unsigned lds_dst) {
    asm volatile("s_mov_b32 m0, %2\n\ts_nop 3\n\tglobal_load_lds_dwordx4 %0, %1 offset:%3" :: "v"(voff), "s"(sbase), "s"(lds_dst), "i"(OFF) : "memory");
}
DI void glds4(unsigned voff, const void* sbase, unsigned lds_dst) {
    unsigned sv;
    asm volatile("s_mov_b32 %0, m0\n\ts_mov_b32 m0, %3\n\ts_nop 2\n\tglobal_load_lds_dword %1, %2\n\ts_mov_b32 m0, %0"
                 : "=&s"(sv) : "v"(voff), "s"(sbase), "s"(lds_dst) : "memory");
}
template <int OFF> DI void gld16(bf16x8& dst, unsigned voff, const void* sbase) {
    asm volatile("s_nop 4\n\tglobal_load_dwordx4 %0, %1, %2 offset:%3" : "=v"(dst) : "v"(voff), "s"(sbase), "i"(OFF) : "memory");
}
template <int OFF, class T> DI void gld16s(T& dst, unsigned voff, const void* sbase) {
    asm volatile("s_nop 4\n\tglobal_load_dwordx4 %0, %1, %2 offset:%3 sc1" : "=v"(dst) : "v"(voff), "s"(sbase), "i"(OFF) : "memory");
}
template <int OFF, bool SC1 = false, class T> DI void gst16(unsigned voff, T v, void* sbase) {
    if constexpr (SC1) asm volatile("s_nop 4\n\tglobal_store_dwordx4 %0, %1, %2 offset:%3 sc1\n\ts_nop 1" :: "v"(voff), "v"(v), "s"(sbase), "i"(OFF) : "memory");
    else asm volatile("s_nop 4\n\tglobal_store_dwordx4 %0, %1, %2 offset:%3\n\ts_nop 1" :: "v"(voff), "v"(v), "s"(sbase), "i"(OFF) : "memory");
}
template <bool SC1 = false, class T> DI void gst_frag4(unsigned voff, void* sbase, T f0, T f1, T f2, T f3) {
    gst16<0, SC1>(voff, f0, sbase); gst16<1024, SC1>(voff, f1, sbase); gst16<2048, SC1>(voff, f2, sbase); gst16<3072, SC1>(voff, f3, sbase);
}
DI void arrive(unsigned* cnt) { __hip_atomic_fetch_add(cnt, 1u, __ATOMIC_RELAXED, __HIP_MEMORY_SCOPE_AGENT); }
DI void wait_count(unsigned* cnt, unsigned target) {
    for (unsigned spins = 0; spins < (1u << 20); ++spins) {
        if ((unsigned)__builtin_amdgcn_readfirstlane(__hip_atomic_load(cnt, __ATOMIC_RELAXED, __HIP_MEMORY_SCOPE_AGENT)) >= target) break;
        __builtin_amdgcn_s_sleep(2);
    }
    asm volatile("" ::: "memory");
}
DI int lane_fresh() { int l; asm volatile("v_mbcnt_lo_u32_b32 %0, -1, 0\n\tv_mbcnt_hi_u32_b32 %0, -1, %0" : "=v"(l)); return l; }
template <int N> DI void waitv() { asm volatile("s_waitcnt vmcnt(%0)" :: "i"(N) : "memory"); }
template <int N> DI void waitv4(bf16x8& a, bf16x8& b, bf16x8& c, bf16x8& d) {
    asm volatile("s_waitcnt vmcnt(%4)" : "+v"(a), "+v"(b), "+v"(c), "+v"(d) : "i"(N) : "memory");
}
DI void bar() { asm volatile("s_waitcnt lgkmcnt(0)\n\ts_barrier" ::: "memory"); }
#define SB() __builtin_amdgcn_sched_barrier(0)
#define PIN(x) asm volatile("" : "+v"(x))
DI void rawbar() { SB(); asm volatile("s_barrier" ::: "memory"); SB(); }
constexpr int PD = 3;
constexpr int PDA = 4;
DI s16x4 trrd(LAS const char* p) { return __builtin_bit_cast(s16x4, __builtin_amdgcn_ds_read_tr16_b64_v4i16((LAS v4i16_t*)p)); }
DI bf16x8 trfrag(LAS const char* p, int off_lo, int off_hi) {
    s16x4 a = trrd(p + off_lo), b = trrd(p + off_hi);
    return (bf16x8){a[0], a[1], a[2], a[3], b[0], b[1], b[2], b[3]};
}
DI bf16x8 ldsfrag(LAS const char* p) { return *(LAS const bf16x8*)p; }
template <class T> DI T* opaque(T* p) { asm volatile("" : "+v"(p)); return p; }
DI bf16x8 pack8(const f32x16& v, int b, float s) {
    u32x4 w = {cvtpk(v[b] * s, v[b + 1] * s), cvtpk(v[b + 2] * s, v[b + 3] * s), cvtpk(v[b + 4] * s, v[b + 5] * s), cvtpk(v[b + 6] * s, v[b + 7] * s)};
    return __builtin_bit_cast(bf16x8, w);
}
DI bf16x8 pack8n(const f32x16& v, int b) {
    u32x4 w = {cvtpk(v[b], v[b + 1]), cvtpk(v[b + 2], v[b + 3]), cvtpk(v[b + 4], v[b + 5]), cvtpk(v[b + 6], v[b + 7])};
    return __builtin_bit_cast(bf16x8, w);
}
DI float xhalf_max(float v) {
    auto rr = __builtin_amdgcn_permlane32_swap(__float_as_uint(v), __float_as_uint(v), false, false);
    return fmaxf(__uint_as_float(rr[0]), __uint_as_float(rr[1]));
}
DI float xhalf_sum(float v) {
    auto rr = __builtin_amdgcn_permlane32_swap(__float_as_uint(v), __float_as_uint(v), false, false);
    return __uint_as_float(rr[0]) + __uint_as_float(rr[1]);
}

template <int CTRL> DI float dpp_add(float v) {
    return v + __uint_as_float(__builtin_amdgcn_update_dpp(0u, __float_as_uint(v), CTRL, 0xf, 0xf, true));
}
DI float wave_sum(float v) {
    v = dpp_add<0xB1>(v); v = dpp_add<0x4E>(v); v = dpp_add<0x141>(v); v = dpp_add<0x140>(v);
    auto r16 = __builtin_amdgcn_permlane16_swap(__float_as_uint(v), __float_as_uint(v), false, false);
    v = __uint_as_float(r16[0]) + __uint_as_float(r16[1]);
    return xhalf_sum(v);
}

__global__ __launch_bounds__(512, 2) void k_kv(const u16* __restrict__ x, const u16* __restrict__ ckv, const u16* __restrict__ wuk,
                                               const u16* __restrict__ wuv, u16* __restrict__ xn, u16* __restrict__ Kf, u16* __restrict__ Vf,
                                               unsigned* __restrict__ cnt) {
    __shared__ __attribute__((aligned(1024))) char lds_[163840];
    const int tid = threadIdx.x, lane = tid & 63, r32 = lane & 31, hi = lane >> 5;
    const int w = __builtin_amdgcn_readfirstlane(tid >> 6);
    const int b = blockIdx.x, xcd = b & 7, idx = b >> 3, h = xcd * 4 + (idx & 3), j = idx >> 2;
    const unsigned lds0 = (unsigned)(uintptr_t)lds_;
    LAS const char* lds = (LAS const char*)lds_;
    bf16x8 xrow;
    gld16<0>(xrow, (unsigned)(lane * 16), (const char*)x + (size_t)(b * 8 + w) * 1024);
    const unsigned wvoff = (unsigned)((8 * w + ((lane >> 2) & 7)) * 8192 + (lane >> 5) * 64 + (lane & 3) * 16);
    const unsigned avoff0 = (unsigned)((lane >> 3) * 1024 + (((lane & 7) ^ (lane >> 4)) * 16));
    const unsigned avoff1 = (unsigned)((lane >> 3) * 1024 + (((lane & 7) ^ (4 + (lane >> 4))) * 16));
    const char* abase = (const char*)ckv + (size_t)(j * 256 + w * 32) * 1024;
    const char* wkb = (const char*)wuk + h * 256;
    const char* wvb = (const char*)wuv + h * 256;
    const unsigned ldst = lds0 + w * 2048, adst = lds0 + 65536 + w * 12288;
    bf16x8 a[32];
    auto issue_p = [&](auto ic, auto pc) __attribute__((always_inline)) {
        constexpr int st = ic, p = pc;
        if constexpr (st < 16) {
            if constexpr (p < 2) glds16(wvoff, (st < 8 ? wkb : wvb) + (size_t)(st & 7) * 64 * 8192 + p * 128, ldst + (st & 3) * 16384 + p * 1024);
            else if constexpr (st < 8) glds16((p & 1) ? avoff1 : avoff0, abase + st * 128 + (p - 2) * 8192, adst + (st % 3) * 4096 + (p - 2) * 1024);
        }
    };
    sfor<0, 3>([&](auto ic) __attribute__((always_inline)) { sfor<0, 6>([&](auto pc) __attribute__((always_inline)) { issue_p(ic, pc); }); });
    const int g16 = lane >> 4, q4 = (lane >> 2) & 3, p4 = lane & 3;
    LAS const char* rb = lds + hi * 2048 + q4 * 64 + (g16 & 1) * 32 + p4 * 8;
    LAS const char* ra[4];
#pragma unroll
    for (int u = 0; u < 4; ++u) ra[u] = opaque(lds + 65536 + w * 12288 + r32 * 128 + (((2 * u + hi) ^ ((r32 >> 1) & 7)) * 16));
    f32x16 acc[4];
    const int rg = 8 * j + w;
    bf16x8 wc[PD];
    auto rdf = [&](int st, int i) __attribute__((always_inline)) {
        const int off = (st & 3) * 16384 + (i >> 2) * 4096 + (i & 3) * 512;
        return trfrag(rb, off, off + 256);
    };
    {
        asm volatile("s_waitcnt vmcnt(18)" : "+v"(xrow) :: "memory");
        u32x4 xu = __builtin_bit_cast(u32x4, xrow);
        float v[8];
#pragma unroll
        for (int e = 0; e < 4; ++e) { v[2 * e] = bf_lo(xu[e]); v[2 * e + 1] = bf_hi(xu[e]); }
        float s = 0.f;
#pragma unroll
        for (int e = 0; e < 8; ++e) s += v[e];
        s = wave_sum(s);
        const float mean = s * (1.0f / DM);
        float q = 0.f;
#pragma unroll
        for (int e = 0; e < 8; ++e) { const float d = v[e] - mean; q += d * d; }
        q = wave_sum(q);
        const float mb = rbf(mean), vb = rbf(q * (1.0f / DM));
        const float sd = rbf(__builtin_sqrtf(rbf(vb + 1e-5f))), rs = 1.0f / sd;
        u32x4 o;
#pragma unroll
        for (int e = 0; e < 4; ++e) o[e] = cvtpk(rbf(v[2 * e] - mb) * rs, rbf(v[2 * e + 1] - mb) * rs);
        const int row = b * 8 + w;
        *(u32x4*)((char*)xn + (size_t)(row >> 5) * 32768 + lane * 512 + (row & 31) * 16) = o;
    }
    if (b == 0 && tid < 48) cnt[tid * 64] = 0u;
    waitv<12>(); rawbar();
#pragma unroll
    for (int u = 0; u < 4; ++u) a[u] = ldsfrag(ra[u]);
#pragma unroll
    for (int i = 0; i < PD; ++i) wc[i] = rdf(0, i);
    sfor<0, 16>([&](auto ic) __attribute__((always_inline)) {
        constexpr int st = ic;
        constexpr int cnt = st <= 5 ? 6 : st <= 7 ? 2 : st <= 9 ? 10 : st <= 13 ? 2 : 0;
        if constexpr (st < 15) { waitv<cnt>(); rawbar(); }
        bf16x8 wf[16 + PD];
#pragma unroll
        for (int i = 0; i < PD; ++i) wf[i] = wc[i];
        sfor<0, 16>([&](auto iic) __attribute__((always_inline)) {
            constexpr int i = iic;
            if constexpr (i + PD < 16) wf[i + PD] = rdf(st, i + PD);
            else if constexpr (st < 15) wf[i + PD] = rdf(st + 1, i + PD - 16);
            if constexpr (st < 7 && i >= 8 && i < 12) a[4 * (st + 1) + i - 8] = ldsfrag(ra[i - 8] + ((st + 1) % 3) * 4096);
            const f32x16 cin = ((st & 7) == 0 && i < 4) ? f32x16{} : acc[i & 3];
            if constexpr (st < 8) acc[i & 3] = MFMA(wf[i], a[4 * (st & 7) + (i >> 2)], cin);
            else acc[i & 3] = MFMA(a[4 * (st & 7) + (i >> 2)], wf[i], cin);
            if constexpr (i >= 1 && i < 7) issue_p(std::integral_constant<int, st + 3>{}, std::integral_constant<int, i - 1>{});
            SB();
        });
#pragma unroll
        for (int i = 0; i < PD; ++i) wc[i] = wf[16 + i];
        if constexpr (st == 7) {
            char* dst = (char*)Kf + (size_t)(h * 64 + rg) * 8192;
            gst_frag4((unsigned)(lane * 16), dst, pack8n(acc[0], 0), pack8n(acc[0], 8), pack8n(acc[1], 0), pack8n(acc[1], 8));
            gst_frag4((unsigned)(lane * 16), dst + 4096, pack8n(acc[2], 0), pack8n(acc[2], 8), pack8n(acc[3], 0), pack8n(acc[3], 8));
        }
        if constexpr (st == 15) {
            char* dst = (char*)Vf + (size_t)(h * 64 + rg) * 8192;
            gst_frag4((unsigned)(lane * 16), dst, pack8n(acc[0], 0), pack8n(acc[1], 0), pack8n(acc[2], 0), pack8n(acc[3], 0));
            gst_frag4((unsigned)(lane * 16), dst + 4096, pack8n(acc[0], 8), pack8n(acc[1], 8), pack8n(acc[2], 8), pack8n(acc[3], 8));
        }
    });
}

constexpr int A_K = 0, A_V = 65536, A_Q = 131072, A_LDS = 163840;

DI float max3f(float a, float b, float c) { return __builtin_fmaxf(__builtin_fmaxf(a, b), c); }
DI void cmask(f32x16& p0, f32x16& p1, int dq) {
    const float NEG = -__builtin_inff();
#pragma unroll
    for (int r = 0; r < 16; ++r) {
        const int c = (r & 3) + 8 * (r >> 2);
        if (c > dq) p0[r] = NEG;
        if (c + 32 > dq) p1[r] = NEG;
    }
}

__global__ __launch_bounds__(512, 2) void k_main(const u16* __restrict__ xn, const u16* __restrict__ wq, const u16* __restrict__ w1,
                                                 const u16* __restrict__ b1, const u16* __restrict__ Kf, const u16* __restrict__ Vf,
                                                 u16* Xf, const u16* __restrict__ wo, const u16* __restrict__ w2, float* P,
                                                 unsigned* cnt, const u16* __restrict__ x, const u16* __restrict__ b2, u16* __restrict__ out) {
    __shared__ __attribute__((aligned(1024))) char lds_[A_LDS];
    const int tid = threadIdx.x, lane = tid & 63, r32 = lane & 31, hi = lane >> 5;
    const int w = __builtin_amdgcn_readfirstlane(tid >> 6);
    const int b = blockIdx.x, xcd = b & 7, idx = b >> 3, h = xcd * 4 + (idx & 3), pi = idx >> 2;
    const int grp = w >> 2, wr = w & 3;
    const int qbig = 15 - pi, qsm = pi;
    const unsigned lds0 = (unsigned)(uintptr_t)lds_;
    LAS char* lds = (LAS char*)lds_;
    const int n1 = 2 * pi + 2;
    const int hl = idx & 3, hc0 = 224 * xcd + (hl < 3 ? 64 * hl : 160);
    const int rg_own = (grp == 0 ? qbig : qsm) * 4 + wr;
    const unsigned wvoff = (unsigned)((8 * w + ((lane >> 2) & 7)) * 8192 + (lane >> 5) * 64 + (lane & 3) * 16);
    const unsigned hvoff = (unsigned)((16 * w + ((lane >> 2) & 7)) * (FF * 2) + (lane >> 5) * 64 + (lane & 3) * 16);
    const unsigned tvoff = (unsigned)(lane * 16);
    const char* abase = (const char*)xn + (size_t)rg_own * 32768;
    const char* wqb = (const char*)wq + h * 256;
    const char* w1b = (const char*)w1 + hc0 * 2;
    const char* kgb = (const char*)Kf + (size_t)h * 32 * 16384;
    const char* vgb = (const char*)Vf + (size_t)h * 32 * 16384;
    const unsigned ldst = lds0 + w * 2048;
    auto issue_step = [&](const char* gb, unsigned region, int s, auto pc) __attribute__((always_inline)) {
        constexpr int p = pc;
        if (s <= 16 && (p == 0 || s >= n1)) {
            const int off = s < n1 ? s * 16384 + w * 2048 : (2 * s - n1) * 16384 + w * 4096;
            glds16x2<2048 * p>(tvoff, gb + off, lds0 + region + (unsigned)(off & 65535));
        }
    };
    constexpr std::integral_constant<int, 0> P0{}; constexpr std::integral_constant<int, 1> P1{};
    bf16x8 qf[8];
    {
        bf16x8 a[32];
        auto issue_p = [&](auto ic, auto pc) __attribute__((always_inline)) {
            constexpr int st = ic, p = pc;
            if constexpr (st < 8) {
                if constexpr (p < 2) glds16(wvoff, wqb + (size_t)st * 64 * 8192 + p * 128, ldst + A_V + (st & 3) * 16384 + p * 1024);
                else gld16<(p - 2) * 1024>(a[4 * st + p - 2], tvoff, abase + st * 4096);
            } else if constexpr (st < 12) {
                if constexpr (p < 2) glds16(hvoff, w1b + (size_t)((st - 8) * 128 + 8 * p) * (FF * 2), ldst + A_V + (st & 3) * 16384 + p * 1024);
                if constexpr (st == 11 && p == 2) glds4((unsigned)(r32 * 4), (const char*)b1 + hc0 * 2, lds0 + A_K + 3 * 16384 + w * 256);
            } else if constexpr (st < 14) {
                if constexpr (p == 0) issue_step(kgb, A_K, st - 12, P0);
            }
        };
        sfor<0, 3>([&](auto ic) __attribute__((always_inline)) { sfor<0, 6>([&](auto pc) __attribute__((always_inline)) { issue_p(ic, pc); }); });
        const int g16 = lane >> 4, q4 = (lane >> 2) & 3, p4 = lane & 3;
        LAS const char* rbq = opaque(lds + A_V + hi * 2048 + q4 * 64 + (g16 & 1) * 32 + p4 * 8);
        LAS const char* rbh = opaque(lds + A_V + hi * 1024 + q4 * 64 + (g16 & 1) * 32 + p4 * 8);
        auto rdf = [&](int st, int i) __attribute__((always_inline)) {
            if (st < 8) { const int off = (st & 3) * 16384 + (i >> 2) * 4096 + (i & 3) * 512; return trfrag(rbq, off, off + 256); }
            else { const int off = (st & 3) * 16384 + (i >> 1) * 2048 + (i & 1) * 512; return trfrag(rbh, off, off + 256); }
        };
        f32x16 acc[4];
        bf16x8 wc[PD];
        waitv<12>(); rawbar();
#pragma unroll
        for (int i = 0; i < PD; ++i) wc[i] = rdf(0, i);
        sfor<0, 12>([&](auto ic) __attribute__((always_inline)) {
            constexpr int st = ic;
            constexpr int cnt = st <= 5 ? 6 : st <= 8 ? 2 : st == 9 ? 3 : st == 10 ? 2 : 4;
            if constexpr (st < 8) waitv4<cnt>(a[4 * st], a[4 * st + 1], a[4 * st + 2], a[4 * st + 3]); else waitv<cnt>();
            rawbar();
            bf16x8 wf[16 + PD];
#pragma unroll
            for (int i = 0; i < PD; ++i) wf[i] = wc[i];
            sfor<0, 16>([&](auto iic) __attribute__((always_inline)) {
                constexpr int i = iic;
                if constexpr (i + PD < 16) wf[i + PD] = rdf(st, i + PD);
                else if constexpr (st < 11) wf[i + PD] = rdf(st + 1, i + PD - 16);
                if constexpr (st < 8) {
                    const f32x16 cin = (st == 0 && i < 4) ? f32x16{} : acc[i & 3];
                    acc[i & 3] = MFMA(wf[i], a[4 * st + (i >> 2)], cin);
                } else {
                    const f32x16 cin = (st == 8 && i < 2) ? f32x16{} : acc[i & 1];
                    acc[i & 1] = MFMA(wf[i], a[8 * (st - 8) + (i >> 1)], cin);
                }
                if constexpr (i >= 1 && i < 7) issue_p(std::integral_constant<int, st + 3>{}, std::integral_constant<int, i - 1>{});
                SB();
            });
#pragma unroll
            for (int i = 0; i < PD; ++i) wc[i] = wf[16 + i];
            if constexpr (st == 7) {
#pragma unroll
                for (int nt = 0; nt < 4; ++nt) { qf[2 * nt] = pack8(acc[nt], 0, QSCALE); qf[2 * nt + 1] = pack8(acc[nt], 8, QSCALE); }
            }
        });
#pragma unroll
        for (int nt = 0; nt < 2; ++nt)
#pragma unroll
            for (int g = 0; g < 4; ++g) {
                const u32x2 bb = *(LAS const u32x2*)(lds + A_K + 3 * 16384 + w * 256 + hi * 8 + (nt * 4 + g) * 16);
                const float bv[4] = {bf_lo(bb[0]), bf_hi(bb[0]), bf_lo(bb[1]), bf_hi(bb[1])};
#pragma unroll
                for (int e = 0; e < 4; ++e) { const float t = fmaxf(acc[nt][4 * g + e] + bv[e], 0.f); acc[nt][4 * g + e] = t * t; }
            }
        char* hdst = (char*)Xf + ((size_t)rg_own * KST + 256 + (hc0 >> 4)) * 1024;
        if (hl < 3) { gst16<0, true>(tvoff, pack8n(acc[0], 0), hdst); gst16<1024, true>(tvoff, pack8n(acc[0], 8), hdst); }
        gst16<2048, true>(tvoff, pack8n(acc[1], 0), hdst); gst16<3072, true>(tvoff, pack8n(acc[1], 8), hdst);
    }
    LAS const char* kb0 = lds + A_K + lane * 16;
    LAS const char* vb0 = opaque(lds + A_V + lane * 16);
    LAS char* qx = opaque(lds + A_Q + wr * 8192 + lane * 16);
    if (grp == 0) {
#pragma unroll
        for (int ks = 0; ks < 8; ++ks) *(LAS bf16x8*)(qx + ks * 1024) = qf[ks];
    }
    const int rg_big = qbig * 4 + wr;
    f32x16 o[4] = {f32x16{}, f32x16{}, f32x16{}, f32x16{}};
    f32x16 negm = f32x16{};
    float mh = 0.f, l = 0.f, il_sm = 0.f;
    int row0 = rg_own * 32;
    f32x16 sA0, sA1, sB0, sB1;
    u32x4 pw[4];
    bf16x8 kc[PDA], vc[PDA];
    bool pend = false; float fpend = 1.f;
    auto tile_of = [&](int s) __attribute__((always_inline)) { return s < n1 ? s : 2 * s - n1 + grp; };
    auto decide = [&](f32x16& c0, f32x16& c1, float rm, bool first) __attribute__((always_inline)) {
        rm = xhalf_max(rm);
        if (__builtin_expect(__any(rm > THR) || first, 0)) {
            const float dl = first ? __builtin_fmaxf(rm, -1e30f) : __builtin_fmaxf(rm, 0.f);
            mh += dl;
#pragma unroll
            for (int r = 0; r < 16; ++r) { c0[r] -= dl; c1[r] -= dl; negm[r] = -mh; }
            fpend = first ? 0.f : __builtin_amdgcn_exp2f(-dl);
            l *= fpend; pend = true;
        }
    };
    waitv<2>(); bar();
    issue_step(vgb, A_V, 0, P0);
    {
#pragma unroll
        for (int ks = 0; ks < 8; ++ks) {
            const bf16x8 k0 = ldsfrag(kb0 + ks * 1024), k1 = ldsfrag(kb0 + 8192 + ks * 1024);
            sA0 = MFMA(k0, qf[ks], ks == 0 ? f32x16{} : sA0);
            sA1 = MFMA(k1, qf[ks], ks == 0 ? f32x16{} : sA1);
        }
        waitv<2>(); bar();
        issue_step(kgb, A_K, 2, P0); issue_step(kgb, A_K, 2, P1);
        if (63 > row0) cmask(sA0, sA1, row0 + r32 - 4 * hi);
        float rm = max3f(sA0[0], sA0[1], sA1[0]);
        rm = __builtin_fmaxf(rm, sA1[1]);
#pragma unroll
        for (int r = 2; r < 16; ++r) rm = max3f(rm, sA0[r], sA1[r]);
        decide(sA0, sA1, rm, true);
        pend = false;
#pragma unroll
        for (int r = 0; r < 16; ++r) { sA0[r] = __builtin_amdgcn_exp2f(sA0[r]); sA1[r] = __builtin_amdgcn_exp2f(sA1[r]); }
#pragma unroll
        for (int i = 0; i < PDA; ++i) kc[i] = ldsfrag(kb0 + 16384 + (i & 1) * 8192 + (i >> 1) * 1024);
        waitv<0>();
        bar();
        if (tid == 0) arrive(cnt + xcd * 64);
    }
    int tV, tK, tKd, tSl;
    {
        const int lf = lane_fresh(), s_ = lf & 31, up = lf >> 5;
        auto tbase = [&](int s) __attribute__((always_inline)) { const int sc = s < 16 ? s : 16, d = sc - n1; return (sc + (d > 0 ? d : 0)) << 14; };
        const int tv = tbase(s_), tk = tbase(s_ + 2), vtwo = s_ >= n1 ? 1 : 0, ktwo = s_ + 2 >= n1 ? 1 : 0;
        tV = tv + (w << (11 + vtwo)) + 2048 * (vtwo & up);
        tK = tk + (w << (11 + ktwo)) + 2048 * (ktwo & up);
        tKd = (int)(lds0 + A_K) + (((s_ + 2 <= 16 ? tk : (tv & ~32767)) & 65535) + (w << (11 + ktwo))) + 2048 * (ktwo & up);
        tSl = (((tv >> 14) + (vtwo & grp)) & 3) << 14;
    }
    const unsigned spec = (1u << 16) | (grp ? (7u << (n1 - 2)) : 0u);
    const int nsw = grp ? n1 : -1;
    LAS const char* kb = kb0 + 16384;
    LAS const char* vb = vb0;
    LAS const char* kbn = kb0 + (tile_of(2) & 3) * 16384;
    auto step = [&](auto evn, f32x16& c0, f32x16& c1, f32x16& p0, f32x16& p1, const int s) __attribute__((always_inline)) {
        waitv<4>();
        rawbar();
        {
            bf16x8 kf[16];
#pragma unroll
            for (int i = 0; i < PDA; ++i) kf[i] = kc[i];
            float sa = 0.f, sb = 0.f;
            unsigned dvo = 0, dm0 = 0;
            sfor<0, 16>([&](auto iic) __attribute__((always_inline)) {
                constexpr int i = iic;
                if constexpr (i + PDA < 16) kf[i + PDA] = ldsfrag(kb + ((i + PDA) & 1) * 8192 + ((i + PDA) >> 1) * 1024);
                else vc[i + PDA - 16] = ldsfrag(vb + (i + PDA - 16) * 1024);
                SB();
                if constexpr (i & 1) c1 = MFMA(kf[i], qf[i >> 1], i < 2 ? negm : c1);
                else c0 = MFMA(kf[i], qf[i >> 1], i < 2 ? negm : c0);
                const float e0 = i < 8 ? p0[2 * i] : p1[2 * i - 16], e1 = i < 8 ? p0[2 * i + 1] : p1[2 * i - 15];
                pw[i >> 2][i & 3] = cvtpk(e0, e1);
                sa += e0; sb += e1; PIN(sa); PIN(sb);
                PIN(pw[i >> 2]);
                if constexpr ((i & 7) == 1) { const int off = __builtin_amdgcn_readlane(tV, i < 8 ? s : s + 32);
                    dvo = tvoff + off; dm0 = lds0 + A_V + (off & 65535); PIN(dvo); }
                if constexpr ((i & 3) == 2) gldsl<1024 * ((i >> 2) & 1)>(dvo, vgb, dm0);
                SB();
            });
            l += sa + sb;
        }
        waitv<4>();
        rawbar();
        const bool sw = (bool)evn && s == nsw;
        const bool swn = !(bool)evn && s + 1 == nsw;
        if (__builtin_expect((spec >> s) & 1, 0)) {
            const int lf = lane_fresh(); cmask(c0, c1, row0 + (lf & 31) - 64 * tile_of(s) - 4 * (lf >> 5));
            if (sw) {
                il_sm = 1.0f / xhalf_sum(l); l = 0.f;
                float rm = max3f(c0[0], c0[1], c1[0]); rm = __builtin_fmaxf(rm, c1[1]);
#pragma unroll
                for (int r = 2; r < 16; ++r) rm = max3f(rm, c0[r], c1[r]);
                decide(c0, c1, rm, true);
            }
        }
        float ra = -__builtin_inff();
        {
            bf16x8 vf[16];
#pragma unroll
            for (int i = 0; i < PDA; ++i) vf[i] = vc[i];
            unsigned dvo = 0, dm0 = 0;
            sfor<0, 16>([&](auto iic) __attribute__((always_inline)) {
                constexpr int i = iic;
                if constexpr (i + PDA < 16) vf[i + PDA] = ldsfrag(vb + (i + PDA) * 1024);
                else kc[i + PDA - 16] = ldsfrag(kbn + ((i + PDA - 16) & 1) * 8192 + ((i + PDA - 16) >> 1) * 1024);
                SB();
                o[i & 3] = MFMA(vf[i], __builtin_bit_cast(bf16x8, pw[i >> 2]), o[i & 3]);
                f32x16& c = i < 8 ? c0 : c1;
                constexpr int e = (2 * i) & 15;
                ra = max3f(ra, c[e], c[e + 1]);
                c[e] = __builtin_amdgcn_exp2f(c[e]); c[e + 1] = __builtin_amdgcn_exp2f(c[e + 1]);
                PIN(ra); PIN(c);
                if constexpr ((i & 7) == 1) { const int off = __builtin_amdgcn_readlane(tK, i < 8 ? s : s + 32);
                    dvo = tvoff + off; dm0 = (unsigned)__builtin_amdgcn_readlane(tKd, i < 8 ? s : s + 32); PIN(dvo); }
                if constexpr ((i & 3) == 2) gldsl<1024 * ((i >> 2) & 1)>(dvo, kgb, dm0);
                if constexpr (i == 15 - PDA) { vb = vb0 + __builtin_amdgcn_readlane(tSl, s); PIN(vb); }
                if constexpr (i == 15) { kb = kbn; kbn = kb0 + __builtin_amdgcn_readlane(tSl, s + 2); PIN(kb); PIN(kbn); }
                SB();
            });
        }
        if (__builtin_expect(__any(ra > THR) || pend || swn, 0)) {
            if (sw) {
                char* dst = (char*)Xf + ((size_t)rg_own * KST + 8 * h) * 1024;
                gst_frag4<true>(tvoff, dst, pack8(o[0], 0, il_sm), pack8(o[0], 8, il_sm), pack8(o[1], 0, il_sm), pack8(o[1], 8, il_sm));
                gst_frag4<true>(tvoff, dst + 4096, pack8(o[2], 0, il_sm), pack8(o[2], 8, il_sm), pack8(o[3], 0, il_sm), pack8(o[3], 8, il_sm));
            }
            if (__any(ra > THR)) {
                const float rm = xhalf_max(ra), dl = __builtin_fmaxf(rm, 0.f);
                fpend = __builtin_amdgcn_exp2f(-dl); pend = true;
                if (__any(rm > OVF)) {
                    const int lf = lane_fresh();
                    const bf16x8* kt = (const bf16x8*)(kgb + (size_t)tile_of(s) * 16384) + lf;
                    c0 = negm; c1 = negm;
#pragma unroll
                    for (int ks = 0; ks < 8; ++ks) { c0 = MFMA(kt[ks * 64], qf[ks], c0); c1 = MFMA(kt[512 + ks * 64], qf[ks], c1); }
                    cmask(c0, c1, row0 + (lf & 31) - 64 * tile_of(s) - 4 * (lf >> 5));
#pragma unroll
                    for (int r = 0; r < 16; ++r) { c0[r] = __builtin_amdgcn_exp2f(c0[r] - dl); c1[r] = __builtin_amdgcn_exp2f(c1[r] - dl); }
                } else {
#pragma unroll
                    for (int r = 0; r < 16; ++r) { c0[r] *= fpend; c1[r] *= fpend; }
                }
                mh += dl; l *= fpend;
#pragma unroll
                for (int r = 0; r < 16; ++r) negm[r] = -mh;
            }
            if (pend) {
#pragma unroll
                for (int d = 0; d < 4; ++d)
#pragma unroll
                    for (int r = 0; r < 16; ++r) o[d][r] *= fpend;
                pend = false;
            }
            if (swn) {
#pragma unroll
                for (int ks = 0; ks < 8; ++ks) qf[ks] = *(LAS const bf16x8*)(qx + ks * 1024);
                negm = f32x16{}; mh = 0.f; row0 = rg_big * 32;
            }
        }
    };
    unsigned dummy = 0;
    for (int s = 1; s < 17; s += 2) {
        if (s == 9) {
            const int lf = lane_fresh(), r = pi * 92 + w * 12 + (lf >> 1), rr = (lf >> 1) < 12 && r < 736 ? r : pi * 92;
            const char* wb = rr < 224 ? (const char*)w2 + (size_t)xcd * 224 * 1024 : (const char*)wo + (size_t)xcd * 512 * 1024 - 224 * 1024;
            asm volatile("global_load_dword %0, %1, off" : "+v"(dummy) : "v"(wb + (size_t)rr * 1024 + hl * 256 + (lf & 1) * 128) : "memory");
        }
        if (s == 15 && w == 0) wait_count(cnt + xcd * 64, 32);
        step(std::false_type{}, sB0, sB1, sA0, sA1, s);
        step(std::true_type{}, sA0, sA1, sB0, sB1, s + 1);
    }
    {
        const int lane = lane_fresh(), hi = lane >> 5;
        const int xs = xcd, rb = idx >> 2, cb = idx & 3;
        const unsigned wvo = (unsigned)((8 * w + ((lane >> 2) & 7)) * 1024 + (lane >> 5) * 64 + (lane & 3) * 16);
        const unsigned wvo_h = (unsigned)((8 * (w & 3) + ((lane >> 2) & 7)) * 1024 + (lane >> 5) * 64 + (lane & 3) * 16);
        const int rg = rb * 8 + w;
        const unsigned avo = (unsigned)(lane * 16);
        const char* xa = (const char*)Xf + (size_t)rg * (KST * 1024) + (size_t)xs * 32 * 1024;
        const char* xh = (const char*)Xf + (size_t)rg * (KST * 1024) + (size_t)(256 + 14 * xs) * 1024;
        const char* wob = (const char*)wo + (size_t)xs * 512 * 1024 + cb * 256;
        const char* w2b = (const char*)w2 + (size_t)xs * 224 * 1024 + cb * 256;
        bf16x8 a[3][4];
        auto issueW = [&](auto ic) __attribute__((always_inline)) {
            constexpr int st = ic;
            const char* src = st < 4 ? w2b + (size_t)st * 65536 : wob + (size_t)(st - 4) * 65536;
            const unsigned vo = st == 3 ? wvo_h : wvo;
            glds16(vo, src, ldst + A_K + (st & 3) * 16384);
            glds16(vo, src + 128, ldst + A_K + (st & 3) * 16384 + 1024);
        };
        auto issueA = [&](auto ic) __attribute__((always_inline)) {
            constexpr int st = ic;
            const char* src = st < 4 ? xh + st * 4096 : xa + (st - 4) * 4096;
            gld16s<0>(a[st % 3][0], avo, src);
            gld16s<1024>(a[st % 3][1], avo, src);
            if constexpr (st != 3) { gld16s<2048>(a[st % 3][2], avo, src); gld16s<3072>(a[st % 3][3], avo, src); }
        };
        asm volatile("s_waitcnt vmcnt(0)" : "+v"(dummy) :: "memory"); bar();
        issueW(std::integral_constant<int, 0>{}); issueA(std::integral_constant<int, 0>{});
        issueW(std::integral_constant<int, 1>{}); issueA(std::integral_constant<int, 1>{});
        issueW(std::integral_constant<int, 2>{});
        {
            float sa = 0.f;
#pragma unroll
            for (int r = 0; r < 16; ++r) sa += sA0[r] + sA1[r];
            l += sa;
            const bf16x8 pf[4] = {pack8n(sA0, 0), pack8n(sA0, 8), pack8n(sA1, 0), pack8n(sA1, 8)};
#pragma unroll
            for (int i = 0; i < 16; ++i) o[i & 3] = MFMA(ldsfrag(vb + i * 1024), pf[i >> 2], o[i & 3]);
        }
        bar();
        l = xhalf_sum(l);
        {
            LAS char* mine = opaque(lds + A_V + w * 8192 + lane * 16);
            LAS char* oth = opaque(lds + A_V + (w ^ 4) * 8192 + lane * 16);
            LAS float* ml = opaque((LAS float*)(lds + A_Q) + lane);
#pragma unroll
            for (int dd = 0; dd < 2; ++dd)
#pragma unroll
                for (int g = 0; g < 4; ++g) {
                    const f32x16& sv = grp == 0 ? o[2 + dd] : o[dd];
                    *(LAS f32x4*)(mine + (dd * 4 + g) * 1024) = (f32x4){sv[4 * g], sv[4 * g + 1], sv[4 * g + 2], sv[4 * g + 3]};
                }
            ml[w * 128] = mh; ml[w * 128 + 64] = l;
            bar();
            const float m2 = ml[(w ^ 4) * 128], l2 = ml[(w ^ 4) * 128 + 64];
            const float mn = fmaxf(mh, m2), f1 = __builtin_amdgcn_exp2f(mh - mn), f2 = __builtin_amdgcn_exp2f(m2 - mn);
            const float il = 1.0f / (l * f1 + l2 * f2), g1 = f1 * il, g2 = f2 * il;
            f32x16 sv[2];
#pragma unroll
            for (int dd = 0; dd < 2; ++dd) {
                sv[dd] = grp == 0 ? o[dd] : o[2 + dd];
#pragma unroll
                for (int g = 0; g < 4; ++g) {
                    const f32x4 t = *(LAS const f32x4*)(oth + (dd * 4 + g) * 1024);
#pragma unroll
                    for (int e = 0; e < 4; ++e) sv[dd][4 * g + e] = sv[dd][4 * g + e] * g1 + t[e] * g2;
                }
            }
            gst_frag4<true>(avo, (char*)Xf + ((size_t)rg_big * KST + 8 * h + 4 * grp) * 1024,
                            pack8n(sv[0], 0), pack8n(sv[0], 8), pack8n(sv[1], 0), pack8n(sv[1], 8));
        }
        waitv<0>(); bar();
        if (tid == 0) arrive(cnt + (8 + xcd) * 64);
        const int g16 = lane >> 4, q4 = (lane >> 2) & 3, p4 = lane & 3;
        LAS const char* rbp = opaque(lds + A_K + hi * 256 + q4 * 64 + 8 * (2 * (g16 & 1) + (p4 >> 1) + 4 * (p4 & 1)));
        f32x16 acc[4];
        bf16x8 wc[PD];
        auto rdf = [&](int st, int i) __attribute__((always_inline)) {
            const int off = (st & 3) * 16384 + (i >> 2) * 4096 + (i & 3) * 512;
            return trfrag(rbp, off, off + 2048);
        };
#pragma unroll
        for (int i = 0; i < PD; ++i) wc[i] = rdf(0, i);
        sfor<0, 12>([&](auto ic) __attribute__((always_inline)) {
            constexpr int st = ic;
            constexpr int cnt_ = st == 2 ? 4 : st <= 9 ? 6 : st == 10 ? 4 : 0;
            if constexpr (st == 2) { if (w == 0) wait_count(cnt + (8 + xcd) * 64, 32); }
            waitv4<cnt_>(a[st % 3][0], a[st % 3][1], a[st % 3][2], a[st % 3][3]);
            rawbar();
            if constexpr (st + 2 < 12) issueA(std::integral_constant<int, st + 2>{});
            if constexpr (st + 3 < 12) issueW(std::integral_constant<int, st + 3>{});
            SB();
            constexpr int NF = st == 3 ? 8 : 16;
            bf16x8 wf[16 + PD];
#pragma unroll
            for (int i = 0; i < PD; ++i) wf[i] = wc[i];
#pragma unroll
            for (int i = 0; i < NF; ++i) {
                if (i + PD < NF) wf[i + PD] = rdf(st, i + PD);
                else if (st < 11) wf[i + PD] = rdf(st + 1, i + PD - NF);
                const f32x16 cin = (st == 0 && i < 4) ? f32x16{} : acc[i & 3];
                acc[i & 3] = MFMA(wf[i], a[st % 3][i >> 2], cin);
                SB();
            }
#pragma unroll
            for (int i = 0; i < PD; ++i) wc[i] = wf[NF + i];
        });
        char* pdst = (char*)P + (((size_t)xs * 64 + rg) * 16 + cb * 4) * 2048;
        gst_frag4<true>(avo, pdst, pack8n(acc[0], 0), pack8n(acc[0], 8), pack8n(acc[1], 0), pack8n(acc[1], 8));
        gst_frag4<true>(avo, pdst + 4096, pack8n(acc[2], 0), pack8n(acc[2], 8), pack8n(acc[3], 0), pack8n(acc[3], 8));
        waitv<0>(); bar();
        if (tid == 0) arrive(cnt + (16 + idx) * 64);
        const int row = (rb * 8 + xs) * 32 + (lane & 31), col = cb * 128 + (w >> 1) * 32 + hi * 16 + 8 * (w & 1);
        u32x4 xv = *(const u32x4*)(x + (size_t)row * 512 + col), bv = *(const u32x4*)(b2 + col);
        PIN(xv); PIN(bv);
        if (w == 0) wait_count(cnt + (16 + idx) * 64, 8);
        bar();
        {
            const int rgr = rb * 8 + xs;
            const char* src = (const char*)P + ((size_t)rgr * 16 + cb * 4) * 2048 + w * 1024;
            u32x4 pv[8];
#pragma unroll
            for (int k = 0; k < 8; ++k) gld16s<0>(pv[k], avo, src + (size_t)k * (64 * 16 * 2048));
            asm volatile("s_waitcnt vmcnt(0)" : "+v"(pv[0]), "+v"(pv[1]), "+v"(pv[2]), "+v"(pv[3]), "+v"(pv[4]), "+v"(pv[5]), "+v"(pv[6]), "+v"(pv[7]) :: "memory");
            u32x4 o4;
#pragma unroll
            for (int e = 0; e < 4; ++e) {
                float lo = bf_lo(xv[e]) + bf_lo(bv[e]), hh = bf_hi(xv[e]) + bf_hi(bv[e]);
#pragma unroll
                for (int k = 0; k < 8; ++k) { lo += bf_lo(pv[k][e]); hh += bf_hi(pv[k][e]); }
                o4[e] = cvtpk(lo, hh);
            }
            *(u32x4*)(out + (size_t)row * 512 + col) = o4;
        }
    }
}

extern "C" void kernel_launch(void* const* d_in, const int* in_sizes, int n_in, void* d_out, int out_size, void* d_ws, size_t ws_size,
                              hipStream_t stream) {
    (void)in_sizes; (void)n_in; (void)out_size; (void)ws_size;
    const u16* x = (const u16*)d_in[0];
    const u16* ckv = (const u16*)d_in[1];
    const u16* wq = (const u16*)d_in[2];
    const u16* wuk = (const u16*)d_in[3];
    const u16* wuv = (const u16*)d_in[4];
    const u16* wo = (const u16*)d_in[5];
    const u16* w1 = (const u16*)d_in[7];
    const u16* b1 = (const u16*)d_in[8];
    const u16* w2 = (const u16*)d_in[9];
    const u16* b2 = (const u16*)d_in[10];
    char* ws = (char*)d_ws;
    u16* xn = (u16*)(ws + WS_XN);
    u16* Kf = (u16*)(ws + WS_K);
    u16* Vf = (u16*)(ws + WS_V);
    u16* Xf = (u16*)(ws + WS_X);
    float* P = (float*)(ws + WS_P);
    unsigned* cnt = (unsigned*)(ws + WS_C);
    k_kv<<<256, 512, 0, stream>>>(x, ckv, wuk, wuv, xn, Kf, Vf, cnt);
    k_main<<<256, 512, 0, stream>>>(xn, wq, w1, b1, Kf, Vf, Xf, wo, w2, P, cnt, x, b2, (u16*)d_out);
}
```
